```python
import torch

def _softmax(x, axis=-1):
    return torch.softmax(x, dim=axis)
def _gelu(x):
    return torch.nn.functional.gelu(x, approximate="tanh")
def _silu(x):
    return torch.nn.functional.silu(x)
def _sigmoid(x):
    return torch.sigmoid(x)
def _layernorm(x, axis=-1, eps=1e-5):
    m = x.mean(dim=axis, keepdim=True)
    v = x.var(dim=axis, keepdim=True, unbiased=False)
    return (x - m) / torch.sqrt(v + eps)
def _scatter_add(tgt, idx, src, axis=0):
    return tgt.clone().index_add_(axis, idx, src)
def _segment_sum(src, seg, n):
    out = torch.zeros((n,) + src.shape[1:], dtype=src.dtype)
    return out.index_add_(0, seg, src)
def _pad(x, pw):
    flat = tuple(v for pair in reversed(pw) for v in pair)
    return torch.nn.functional.pad(x, flat)

kernel_name = 'gated_mix__ssm_conv_s4__linear_attn_block__prompt'

def setup_inputs():

    _w = (lambda a: (lambda t: t.to(torch.bfloat16) if t.is_floating_point() else t)(torch.as_tensor(a)))
    def _A_setup():
        B, L, D, N = 1, 2048, 1024, 128
        A_re = -(lambda _x: torch.exp(torch.as_tensor(_x)))((lambda *s: torch.randn(*s, dtype=torch.bfloat16) * 3.2)(D, N))
        A_im = torch.pi * (lambda *s: torch.randn(*s, dtype=torch.bfloat16) * 0.05)(D, N)
        B_re = (lambda *s: torch.randn(*s, dtype=torch.bfloat16) * 0.2)(D, N)
        B_im = (lambda *s: torch.randn(*s, dtype=torch.bfloat16) * 0.2)(D, N)
        C_re = (lambda *s: torch.randn(*s, dtype=torch.bfloat16) * 0.2)(D, N)
        C_im = (lambda *s: torch.randn(*s, dtype=torch.bfloat16) * 0.2)(D, N)
        log_dt = (lambda *s: torch.randn(*s, dtype=torch.bfloat16) * 0.05)(D) * 0.5 - 4.0
        Dskip = (lambda *s: torch.randn(*s, dtype=torch.bfloat16) * 0.1)(D)
        return {
            "u": _w((lambda *s: torch.randn(*s, dtype=torch.bfloat16) * 0.05)(B, L, D)),
            "A_re": _w(A_re),
            "A_im": _w(A_im),
            "B_re": _w(B_re),
            "B_im": _w(B_im),
            "C_re": _w(C_re),
            "C_im": _w(C_im),
            "log_dt": _w(log_dt),
            "Dskip": _w(Dskip),
        }
    _A_in = _A_setup()

    def _B_setup():
        B, S, D = 1, 2048, 1024
        return {
            "x": _w((lambda *s: torch.randn(*s, dtype=torch.bfloat16) * 0.05)(B, S, D)),
            "wqkv": _w((lambda *s: torch.randn(*s, dtype=torch.bfloat16) * 0.8)(D, 3 * D)),
            "wo": _w((lambda *s: torch.randn(*s, dtype=torch.bfloat16) * 0.05)(D, D)),
        }
    _B_in = _B_setup()

    return {
        "A_u": _A_in["u"],
        "A_A_re": _A_in["A_re"],
        "A_A_im": _A_in["A_im"],
        "A_B_re": _A_in["B_re"],
        "A_B_im": _A_in["B_im"],
        "A_C_re": _A_in["C_re"],
        "A_C_im": _A_in["C_im"],
        "A_log_dt": _A_in["log_dt"],
        "A_Dskip": _A_in["Dskip"],
        "B_x": _B_in["x"],
        "B_wqkv": _B_in["wqkv"],
        "B_wo": _B_in["wo"],
        "gate_w": _w((lambda *s: torch.randn(*s, dtype=torch.bfloat16) * 0.05)(1024) * 16.0),
    }

def reference(A_u, A_A_re, A_A_im, A_B_re, A_B_im, A_C_re, A_C_im, A_log_dt, A_Dskip, B_x, B_wqkv, B_wo, gate_w):
    def _A_ref(u, A_re, A_im, B_re, B_im, C_re, C_im, log_dt, Dskip):
        Bsz, L, D = u.shape
        dt = torch.exp(log_dt)[:, None]
        A = A_re + 1j * A_im
        Bc = B_re + 1j * B_im
        Cc = C_re + 1j * C_im
        logdA = dt * A
        dB = dt * Bc
        l = (lambda x, dt: x.to(dt))(torch.arange(L), A_re.dtype)
        P = torch.exp(logdA[:, :, None] * l[None, None, :])
        K = torch.einsum("dn,dnl,dn->dl", Cc, P, dB).real
        L2 = 2 * L
        Kp = _pad(K, ((0, 0), (0, L)))
        up = (lambda x, *d: x.permute(*d))(u, 0, 2, 1)
        up = _pad(up, ((0, 0), (0, 0), (0, L)))
        Kf = (lambda x, axes=(-2,-1): torch.fft.rfft2(x.float(), dim=axes))(Kp[:, None, :], axes=(-2, -1))
        Uf = (lambda x, axes=(-2,-1): torch.fft.rfft2(x.float(), dim=axes))(up[:, :, None, :], axes=(-2, -1))
        Yf = Uf * Kf[None, :, :, :]
        y = (lambda x, s, axes=(-2,-1): torch.fft.irfft2(x, s=s, dim=axes).to(torch.bfloat16))(Yf, (1, L2), axes=(-2, -1))[:, :, 0, :L]
        y = (lambda x, *d: x.permute(*d))(y, 0, 2, 1) + u * Dskip[None, None, :]
        return y

    def _B_ref(x, wqkv, wo):
        B, S, D = x.shape
        H = 32; Dh = D // H
        qkv = (lambda x, *d: x.permute(*d))((x @ wqkv).reshape(B, S, 3, H, Dh), 2, 0, 3, 1, 4)
        q, k, v = qkv[0], qkv[1], qkv[2]
        phi = lambda t: torch.where(t > 0, t + 1, torch.exp(t)) + 1e-6
        pq, pk = phi(q), phi(k)
        kv = torch.einsum("bhsd,bhse->bhde", pk, v)
        z = torch.einsum("bhsd->bhd", pk)
        num = torch.einsum("bhqd,bhde->bhqe", pq, kv)
        den = torch.einsum("bhqd,bhd->bhq", pq, z)[..., None] + 1e-6
        o = num / den
        return (lambda x, *d: x.permute(*d))(o, 0, 2, 1, 3).reshape(B, S, D) @ wo

    x = A_u
    g = _sigmoid((x * gate_w).sum(-1, keepdims=True))
    a = _A_ref(x, A_A_re, A_A_im, A_B_re, A_B_im, A_C_re, A_C_im, A_log_dt, A_Dskip)
    b = _B_ref(x, B_wqkv, B_wo)
    return g * a + (1 - g) * b
```

```cpp
#include <hip/hip_runtime.h>
#include <hip/hip_bf16.h>
#include <stdint.h>

typedef unsigned short u16;
__device__ __forceinline__ float bf2f(u16 v) { return __uint_as_float(((unsigned)v) << 16); }
__device__ __forceinline__ u16 f2bf(float f) {
  unsigned u = __float_as_uint(f);
  if ((u & 0x7fffffffu) > 0x7f800000u) return (u16)((u >> 16) | 0x40);
  u += 0x7fffu + ((u >> 16) & 1u);
  return (u16)(u >> 16);
}
__device__ __forceinline__ float rbf(float f) { return bf2f(f2bf(f)); }

#define S_ 2048
#define D_ 1024
#define N_ 128
#define H_ 32

__global__ void kgen_naive(const u16* Are, const u16* Aim, const u16* Bre, const u16* Bim, const u16* Cre, const u16* Cim,
                           const u16* logdt, float* K) {
  int d = blockIdx.x;
  __shared__ float s_ar[N_], s_ai[N_], s_cr[N_], s_ci[N_];
  float dt = rbf(expf(bf2f(logdt[d])));
  if (threadIdx.x < N_) {
    int n = threadIdx.x;
    float ar = dt * bf2f(Are[d * N_ + n]), ai = dt * bf2f(Aim[d * N_ + n]);
    float br = dt * bf2f(Bre[d * N_ + n]), bi = dt * bf2f(Bim[d * N_ + n]);
    float cr = bf2f(Cre[d * N_ + n]), ci = bf2f(Cim[d * N_ + n]);
    s_ar[n] = ar; s_ai[n] = ai;
    s_cr[n] = cr * br - ci * bi; s_ci[n] = cr * bi + ci * br;
  }
  __syncthreads();
  for (int l = threadIdx.x; l < S_; l += blockDim.x) {
    float lf = rbf((float)l);
    float acc = 0.f;
    for (int n = 0; n < N_; ++n) {
      float e = expf(s_ar[n] * lf);
      float s, c; sincosf(s_ai[n] * lf, &s, &c);
      acc += e * (s_cr[n] * c - s_ci[n] * s);
    }
    K[d * S_ + l] = acc;
  }
}

__global__ void conv_naive(const u16* u, const float* K, const u16* Dskip, u16* a) {
  int d = blockIdx.x * 64 + (threadIdx.x & 63);
  int t = blockIdx.y * 4 + (threadIdx.x >> 6);
  float acc = 0.f;
  for (int l = 0; l <= t; ++l) acc += K[d * S_ + l] * bf2f(u[(t - l) * D_ + d]);
  float y = rbf(acc);
  float ud = rbf(bf2f(u[t * D_ + d]) * bf2f(Dskip[d]));
  a[t * D_ + d] = f2bf(y + ud);
}

template <int KDIM, int NDIM>
__global__ void gemm_naive(const u16* A, const u16* B, u16* C) {
  int n = blockIdx.x * 64 + (threadIdx.x & 63);
  int m = blockIdx.y * 4 + (threadIdx.x >> 6);
  float acc = 0.f;
  for (int k = 0; k < KDIM; ++k) acc += bf2f(A[m * KDIM + k]) * bf2f(B[k * NDIM + n]);
  C[m * NDIM + n] = f2bf(acc);
}

__device__ __forceinline__ float phi(float t) {
  float r = t > 0.f ? rbf(t + 1.f) : rbf(expf(t));
  return rbf(r + 1e-6f);
}

__global__ void kv_naive(const u16* qkv, float* kv, float* z) {
  int h = blockIdx.x;
  int d = threadIdx.x >> 5, e = threadIdx.x & 31;
  float acc = 0.f, zz = 0.f;
  for (int s = 0; s < S_; ++s) {
    float pk = phi(bf2f(qkv[s * 3072 + 1024 + h * 32 + d]));
    float v = bf2f(qkv[s * 3072 + 2048 + h * 32 + e]);
    acc += pk * v; zz += pk;
  }
  kv[h * 1024 + d * 32 + e] = rbf(acc);
  if (e == 0) z[h * 32 + d] = rbf(zz);
}

__global__ void o_naive(const u16* qkv, const float* kv, const float* z, u16* o) {
  int s = blockIdx.x;
  int c = threadIdx.x;
  int h = c >> 5, e = c & 31;
  float num = 0.f, den = 0.f;
  for (int d = 0; d < 32; ++d) {
    float pq = phi(bf2f(qkv[s * 3072 + h * 32 + d]));
    num += pq * kv[h * 1024 + d * 32 + e];
    den += pq * z[h * 32 + d];
  }
  num = rbf(num); den = rbf(rbf(den) + 1e-6f);
  o[s * D_ + c] = f2bf(num / den);
}

__global__ void mix_naive(const u16* u, const u16* gw, const u16* a, const u16* b, u16* out) {
  int s = blockIdx.x;
  __shared__ float red[1024];
  int c = threadIdx.x;
  red[c] = rbf(bf2f(u[s * D_ + c]) * bf2f(gw[c]));
  __syncthreads();
  for (int st = 512; st > 0; st >>= 1) { if (c < st) red[c] += red[c + st]; __syncthreads(); }
  float x = rbf(red[0]);
  float g = rbf(1.f / (1.f + expf(-x)));
  float omg = rbf(1.f - g);
  float r = rbf(g * bf2f(a[s * D_ + c])) + rbf(omg * bf2f(b[s * D_ + c]));
  out[s * D_ + c] = f2bf(r);
}

extern "C" void kernel_launch(void* const* d_in, const int* in_sizes, int n_in,
                              void* d_out, int out_size, void* d_ws, size_t ws_size,
                              hipStream_t stream) {
  const u16* u = (const u16*)d_in[0];
  const u16* Are = (const u16*)d_in[1];
  const u16* Aim = (const u16*)d_in[2];
  const u16* Bre = (const u16*)d_in[3];
  const u16* Bim = (const u16*)d_in[4];
  const u16* Cre = (const u16*)d_in[5];
  const u16* Cim = (const u16*)d_in[6];
  const u16* logdt = (const u16*)d_in[7];
  const u16* Dskip = (const u16*)d_in[8];
  const u16* wqkv = (const u16*)d_in[10];
  const u16* wo = (const u16*)d_in[11];
  const u16* gw = (const u16*)d_in[12];
  u16* out = (u16*)d_out;
  char* ws = (char*)d_ws;
  float* K = (float*)ws;
  u16* a = (u16*)(ws + (8 << 20));
  u16* qkv = (u16*)(ws + (12 << 20));
  float* kv = (float*)(ws + (24 << 20));
  float* z = (float*)(ws + (25 << 20));
  u16* o = (u16*)(ws + (26 << 20));
  u16* b = (u16*)(ws + (30 << 20));
  kgen_naive<<<D_, 256, 0, stream>>>(Are, Aim, Bre, Bim, Cre, Cim, logdt, K);
  conv_naive<<<dim3(D_ / 64, S_ / 4), 256, 0, stream>>>(u, K, Dskip, a);
  gemm_naive<1024, 3072><<<dim3(3072 / 64, S_ / 4), 256, 0, stream>>>(u, wqkv, qkv);
  kv_naive<<<H_, 1024, 0, stream>>>(qkv, kv, z);
  o_naive<<<S_, 1024, 0, stream>>>(qkv, kv, z, o);
  gemm_naive<1024, 1024><<<dim3(1024 / 64, S_ / 4), 256, 0, stream>>>(o, wo, b);
  mix_naive<<<S_, 1024, 0, stream>>>(u, gw, a, b, out);
}
```

```cpp
#include <hip/hip_runtime.h>
#include <stdint.h>

typedef unsigned short u16;
typedef __attribute__((ext_vector_type(8))) short bf16x8;
typedef __attribute__((ext_vector_type(4))) short s16x4;
typedef __attribute__((ext_vector_type(4))) float f32x4;
typedef __attribute__((ext_vector_type(2))) float f32x2;
typedef __attribute__((ext_vector_type(2))) __bf16 bf16x2_t;
typedef __attribute__((ext_vector_type(4))) unsigned u32x4;
typedef __attribute__((ext_vector_type(2))) unsigned u32x2;
typedef __attribute__((address_space(3))) s16x4* lds_s16x4_ptr;

#define NOPK __attribute__((target("no-packed-fp32-ops")))
#define DI __device__ __forceinline__ NOPK
#define U2F(x) __builtin_bit_cast(float, (unsigned)(x))
#define F2U(x) __builtin_bit_cast(unsigned, (float)(x))
#define SYNC()                                                 \
  do {                                                         \
    __builtin_amdgcn_fence(__ATOMIC_RELEASE, "workgroup");     \
    __builtin_amdgcn_s_barrier();                              \
    __builtin_amdgcn_fence(__ATOMIC_ACQUIRE, "workgroup");     \
  } while (0)
#define MFMA16(a, b, c) __builtin_amdgcn_mfma_f32_16x16x32_bf16((a), (b), (c), 0, 0, 0)

#define S_ 2048
#define D_ 1024
#define NST 128
#define KCW 648

DI float bf2f(u16 v) { return U2F(((unsigned)v) << 16); }
DI unsigned pk2(float lo, float hi) {
  f32x2 f = {lo, hi};
  bf16x2_t b = __builtin_convertvector(f, bf16x2_t);
  return __builtin_bit_cast(unsigned, b);
}
DI float rbf(float f) { return U2F(pk2(f, 0.f) << 16); }
DI float lo16(unsigned p) { return U2F(p << 16); }
DI float hi16(unsigned p) { return U2F(p & 0xffff0000u); }

DI float fexp(float x) {
  float t = x * 1.44269504088896340736f;
  float e = __builtin_fmaf(x, 1.44269504088896340736f, -t) + x * 1.92596299112661746e-8f;
  return __builtin_amdgcn_exp2f(t) * (1.f + e * 0.69314718056f);
}
DI float shx(float v, int m) {
  int lane = threadIdx.x & 63;
  return U2F(__builtin_amdgcn_ds_bpermute((lane ^ m) << 2, (int)F2U(v)));
}
DI float phi_f(float t) {
  float r = t > 0.f ? rbf(t + 1.f) : rbf(fexp(t));
  return rbf(r + 1e-6f);
}

DI void cexp_fast(float xr, float xi, float& o_r, float& o_i) {
  float e = __builtin_amdgcn_exp2f(xr * 1.44269504088896340736f);
  float rev = xi * 0.15915494309189533577f;
  rev = __builtin_amdgcn_fractf(rev);
  o_r = e * __builtin_amdgcn_cosf(rev);
  o_i = e * __builtin_amdgcn_sinf(rev);
}

DI void split8(const float* x, bf16x8& hi, bf16x8& lo) {
  u32x4 h, l;
#pragma unroll
  for (int i = 0; i < 4; ++i) {
    unsigned ph = pk2(x[2 * i], x[2 * i + 1]);
    h[i] = ph;
    l[i] = pk2(x[2 * i] - lo16(ph), x[2 * i + 1] - hi16(ph));
  }
  hi = __builtin_bit_cast(bf16x8, h);
  lo = __builtin_bit_cast(bf16x8, l);
}

DI void kgen_wave(int d, int lane, const u16* __restrict__ Are, const u16* __restrict__ Aim, const u16* __restrict__ Bre,
                  const u16* __restrict__ Bim, const u16* __restrict__ Cre, const u16* __restrict__ Cim,
                  const u16* __restrict__ logdt, float* __restrict__ Kc) {
  const float dt = rbf(fexp(bf2f(logdt[d])));
  const int a = lane & 15, g = lane >> 4;
  f32x4 acc[5];
#pragma unroll
  for (int r = 0; r < 5; ++r) acc[r] = (f32x4){0.f, 0.f, 0.f, 0.f};
  const float fa = (float)(16 * a), fb = (float)a;
  for (int ks = 0; ks < 8; ++ks) {
    const int n0 = d * NST + 16 * ks + 4 * g;
    u32x2 vAr = *(const u32x2*)(Are + n0), vAi = *(const u32x2*)(Aim + n0);
    u32x2 vBr = *(const u32x2*)(Bre + n0), vBi = *(const u32x2*)(Bim + n0);
    u32x2 vCr = *(const u32x2*)(Cre + n0), vCi = *(const u32x2*)(Cim + n0);
    float par[4], pai[4], pbr[4], pbi[4], cr[4], ci[4];
#pragma unroll
    for (int i = 0; i < 4; ++i) {
      float ar = (i & 1) ? hi16(vAr[i >> 1]) : lo16(vAr[i >> 1]);
      float ai = (i & 1) ? hi16(vAi[i >> 1]) : lo16(vAi[i >> 1]);
      float br = (i & 1) ? hi16(vBr[i >> 1]) : lo16(vBr[i >> 1]);
      float bi = (i & 1) ? hi16(vBi[i >> 1]) : lo16(vBi[i >> 1]);
      float c_r = (i & 1) ? hi16(vCr[i >> 1]) : lo16(vCr[i >> 1]);
      float c_i = (i & 1) ? hi16(vCi[i >> 1]) : lo16(vCi[i >> 1]);
      ar *= dt; ai *= dt; br *= dt; bi *= dt;
      cr[i] = c_r * br - c_i * bi;
      ci[i] = c_r * bi + c_i * br;
      cexp_fast(ar * fa, ai * fa, par[i], pai[i]);
      cexp_fast(ar * fb, ai * fb, pbr[i], pbi[i]);
    }
#pragma unroll
    for (int r = 0; r < 5; ++r) {
      float xa[8], xb[8];
#pragma unroll
      for (int i = 0; i < 4; ++i) {
        xa[2 * i] = cr[i] * par[i] - ci[i] * pai[i];
        xa[2 * i + 1] = cr[i] * pai[i] + ci[i] * par[i];
        xb[2 * i] = pbr[i];
        xb[2 * i + 1] = -pbi[i];
      }
      bf16x8 ah, al, bh, bl;
      split8(xa, ah, al);
      split8(xb, bh, bl);
      acc[r] = MFMA16(ah, bh, acc[r]);
      acc[r] = MFMA16(ah, bl, acc[r]);
      acc[r] = MFMA16(al, bh, acc[r]);
      if (r < 4) {
#pragma unroll
        for (int i = 0; i < 4; ++i) {
          float tr = par[i] * par[i] - pai[i] * pai[i], ti = 2.f * par[i] * pai[i];
          par[i] = tr; pai[i] = ti;
          tr = pbr[i] * pbr[i] - pbi[i] * pbi[i]; ti = 2.f * pbr[i] * pbi[i];
          pbr[i] = tr; pbi[i] = ti;
        }
      }
    }
  }
  float* out = Kc + (size_t)d * KCW;
#pragma unroll
  for (int j = 0; j < 4; ++j) out[16 * (4 * g + j) + a] = acc[0][j];
  if (g >= 2) {
#pragma unroll
    for (int r = 1; r < 4; ++r)
#pragma unroll
      for (int j = 0; j < 4; ++j) out[256 + (r - 1) * 128 + 16 * (4 * g + j - 8) + a] = acc[r][j];
  }
  if (lane == 32) out[640] = acc[4][0];
}

template <int MT, int NT>
struct GemmCfg {
  static constexpr int BM = 32 * MT;
  static constexpr int NCG = NT;
  static constexpr int A_BYTES = BM * 128;
  static constexpr int B_BYTES = NCG * 8 * 512;
  static constexpr int STAGE = A_BYTES + B_BYTES;
};

template <int MT, int NT, int LDB, typename ColFn>
DI void gemm_core(const u16* __restrict__ A, const u16* __restrict__ B, ColFn colfn, char* smem, f32x4 (&acc)[MT][NT]) {
  using C = GemmCfg<MT, NT>;
  constexpr int NCG = C::NCG;
  const int tid = threadIdx.x, lane = tid & 63, wid = __builtin_amdgcn_readfirstlane(tid >> 6);
  const int wr = wid >> 1, wc = wid & 1;
  const int r16 = lane & 15, g = lane >> 4;
  const u16* asrc[MT];
#pragma unroll
  for (int i = 0; i < MT; ++i) {
    int P = (wid * MT + i) * 64 + lane;
    int line = P >> 4, slot = P & 15;
    int row = 2 * line + (slot >> 3), chunk = (slot & 7) ^ (line & 7);
    asrc[i] = A + (size_t)row * D_ + chunk * 8;
  }
  const u16* bsrc[NT];
#pragma unroll
  for (int i = 0; i < NT; ++i) {
    int ins = wid * NT + i;
    int kb = ins / (NCG / 2), cgp = ins % (NCG / 2);
    int sub = lane >> 5, r7 = (lane & 31) >> 2, chp = lane & 3;
    int row = kb * 8 + r7;
    int ch = chp ^ ((row >> 2) & 3);
    bsrc[i] = B + (size_t)row * LDB + colfn(cgp * 2 + sub) + ch * 8;
  }
#define GSTAGE(buf, kt)                                                                                                 \
  do {                                                                                                                  \
    char* sa_ = smem + (buf) * C::STAGE;                                                                                \
    char* sb_ = sa_ + C::A_BYTES;                                                                                       \
    _Pragma("unroll") for (int i = 0; i < MT; ++i) __builtin_amdgcn_global_load_lds(                                    \
        (const unsigned*)(asrc[i] + (kt) * 64), (unsigned*)(sa_ + (wid * MT + i) * 1024 + lane * 16), 16, 0, 0);        \
    _Pragma("unroll") for (int i = 0; i < NT; ++i) __builtin_amdgcn_global_load_lds(                                    \
        (const unsigned*)(bsrc[i] + (size_t)(kt) * 64 * LDB), (unsigned*)(sb_ + (wid * NT + i) * 1024 + lane * 16), 16, 0, 0); \
  } while (0)
  unsigned aoff[2];
  {
    int row = wr * (16 * MT) + r16;
#pragma unroll
    for (int ks = 0; ks < 2; ++ks)
      aoff[ks] = (row >> 1) * 256 + (((row & 1) * 8) + ((4 * ks + g) ^ ((row >> 1) & 7))) * 16;
  }
  unsigned boff[2][2];
  {
    int q = r16 >> 2, p = lane & 3;
#pragma unroll
    for (int half = 0; half < 2; ++half)
#pragma unroll
      for (int t = 0; t < 2; ++t)
        boff[half][t] = C::A_BYTES + (g * NCG + wc * (NT / 2)) * 512 + (4 * t + q) * 64 +
                        ((2 * (half ^ (g & 1))) + ((p >> 1) ^ t)) * 16 + 8 * (p & 1);
  }
#pragma unroll
  for (int m = 0; m < MT; ++m)
#pragma unroll
    for (int n = 0; n < NT; ++n) acc[m][n] = (f32x4){0.f, 0.f, 0.f, 0.f};

  GSTAGE(0, 0);
  asm volatile("s_waitcnt vmcnt(0)" ::: "memory");
  SYNC();
  for (int kt = 0; kt < 16; ++kt) {
    const int cur = kt & 1;
    if (kt + 1 < 16) GSTAGE(cur ^ 1, kt + 1);
    char* sbuf = smem + cur * C::STAGE;
#pragma unroll
    for (int ks = 0; ks < 2; ++ks) {
      bf16x8 af[MT], bfr[NT];
#pragma unroll
      for (int m = 0; m < MT; ++m) af[m] = *(const bf16x8*)(sbuf + aoff[ks] + m * 2048);
#pragma unroll
      for (int n = 0; n < NT; ++n) {
        const int cg = n >> 1, half = n & 1;
        s16x4 t0 = __builtin_amdgcn_ds_read_tr16_b64_v4i16((lds_s16x4_ptr)(sbuf + boff[half][0] + (4 * ks * NCG + cg) * 512));
        s16x4 t1 = __builtin_amdgcn_ds_read_tr16_b64_v4i16((lds_s16x4_ptr)(sbuf + boff[half][1] + (4 * ks * NCG + cg) * 512));
        bfr[n] = __builtin_shufflevector(t0, t1, 0, 1, 2, 3, 4, 5, 6, 7);
      }
#pragma unroll
      for (int m = 0; m < MT; ++m)
#pragma unroll
        for (int n = 0; n < NT; ++n) acc[m][n] = MFMA16(af[m], bfr[n], acc[m][n]);
    }
    asm volatile("s_waitcnt vmcnt(0)" ::: "memory");
    SYNC();
  }
}

struct QkvCol {
  int hp;
  DI int operator()(int cg) const {
    return (cg % 3) * 1024 + hp * 64 + (cg / 3) * 32;
  }
};

__global__ __launch_bounds__(256, 2) NOPK void k_l1(const u16* __restrict__ u, const u16* __restrict__ wqkv,
                                               const u16* __restrict__ Are, const u16* __restrict__ Aim,
                                               const u16* __restrict__ Bre, const u16* __restrict__ Bim,
                                               const u16* __restrict__ Cre, const u16* __restrict__ Cim,
                                               const u16* __restrict__ logdt, const u16* __restrict__ gw,
                                               float* __restrict__ Kc, u16* __restrict__ uT, u16* __restrict__ pq,
                                               float* __restrict__ kvp, float* __restrict__ gate) {
  extern __shared__ __attribute__((aligned(16))) char smem[];
  const int bid = blockIdx.x;
  const int tid = threadIdx.x, lane = tid & 63, wid = __builtin_amdgcn_readfirstlane(tid >> 6);
  if (bid < 256) {
    const int xcd = bid & 7, i = bid >> 3;
    const int mt = (xcd & 3) * 4 + (i & 3), hp = (xcd >> 2) * 8 + (i >> 2);
    f32x4 acc[4][6];
    QkvCol cf{hp};
    gemm_core<4, 6, 3072>(u + (size_t)mt * 128 * D_, wqkv, cf, smem, acc);
    const int wr = wid >> 1, wc = wid & 1, r16 = lane & 15, g = lane >> 4;
    u16* sq = (u16*)smem;
#pragma unroll
    for (int m = 0; m < 4; ++m)
#pragma unroll
      for (int n = 0; n < 2; ++n)
#pragma unroll
        for (int j = 0; j < 4; ++j) {
          float v = phi_f(rbf(acc[m][n][j]));
          sq[(wr * 64 + m * 16 + 4 * g + j) * 72 + wc * 32 + n * 16 + r16] = (u16)(F2U(v) >> 16);
        }
    f32x4 kvacc[2][2], zacc[2];
#pragma unroll
    for (int x = 0; x < 2; ++x) {
      zacc[x] = (f32x4){0.f, 0.f, 0.f, 0.f};
#pragma unroll
      for (int y = 0; y < 2; ++y) kvacc[x][y] = (f32x4){0.f, 0.f, 0.f, 0.f};
    }
    bf16x8 ones;
#pragma unroll
    for (int j = 0; j < 8; ++j) ones[j] = (short)0x3f80;
#pragma unroll
    for (int mp = 0; mp < 2; ++mp) {
      bf16x8 kf[2], vf[2];
#pragma unroll
      for (int x = 0; x < 2; ++x) {
        u32x4 pk_, pv_;
#pragma unroll
        for (int h = 0; h < 2; ++h) {
          const f32x4 ka = acc[2 * mp + h][2 + x], va = acc[2 * mp + h][4 + x];
          pk_[2 * h] = pk2(phi_f(rbf(ka[0])), phi_f(rbf(ka[1])));
          pk_[2 * h + 1] = pk2(phi_f(rbf(ka[2])), phi_f(rbf(ka[3])));
          pv_[2 * h] = pk2(va[0], va[1]);
          pv_[2 * h + 1] = pk2(va[2], va[3]);
        }
        kf[x] = __builtin_bit_cast(bf16x8, pk_);
        vf[x] = __builtin_bit_cast(bf16x8, pv_);
      }
#pragma unroll
      for (int x = 0; x < 2; ++x) {
#pragma unroll
        for (int y = 0; y < 2; ++y) kvacc[x][y] = MFMA16(kf[x], vf[y], kvacc[x][y]);
        zacc[x] = MFMA16(kf[x], ones, zacc[x]);
      }
    }
    float* sred = (float*)(smem + 20480) + wc * 1056;
    if (wr == 1) {
#pragma unroll
      for (int x = 0; x < 2; ++x) {
#pragma unroll
        for (int y = 0; y < 2; ++y)
#pragma unroll
          for (int j = 0; j < 4; ++j) sred[(x * 16 + 4 * g + j) * 32 + y * 16 + r16] = kvacc[x][y][j];
        if (r16 == 0)
#pragma unroll
          for (int j = 0; j < 4; ++j) sred[1024 + x * 16 + 4 * g + j] = zacc[x][j];
      }
    }
    SYNC();
    if (wr == 0) {
      float* dst = kvp + ((size_t)(hp * 2 + wc) * 16 + mt) * 1056;
#pragma unroll
      for (int x = 0; x < 2; ++x) {
#pragma unroll
        for (int y = 0; y < 2; ++y)
#pragma unroll
          for (int j = 0; j < 4; ++j) {
            int idx = (x * 16 + 4 * g + j) * 32 + y * 16 + r16;
            dst[idx] = kvacc[x][y][j] + sred[idx];
          }
        if (r16 == 0)
#pragma unroll
          for (int j = 0; j < 4; ++j) {
            int idx = 1024 + x * 16 + 4 * g + j;
            dst[idx] = zacc[x][j] + sred[idx];
          }
      }
    }
#pragma unroll
    for (int it = 0; it < 4; ++it) {
      int idx = it * 256 + tid;
      int row = idx >> 3, c8 = idx & 7;
      u32x4 v = *(const u32x4*)(sq + row * 72 + c8 * 8);
      *(u32x4*)(pq + (size_t)(mt * 128 + row) * D_ + hp * 64 + c8 * 8) = v;
    }
  } else if (bid < 512) {
    const int d = (bid - 256) * 4 + wid;
    kgen_wave(d, lane, Are, Aim, Bre, Bim, Cre, Cim, logdt, Kc);
  } else if (bid < 640) {
    u16* st = (u16*)smem;
    for (int tl = 0; tl < 4; ++tl) {
      int tile = (bid - 512) * 4 + tl;
      int tt = tile >> 4, dd = tile & 15;
#pragma unroll
      for (int it = 0; it < 2; ++it) {
        int idx = it * 256 + tid;
        int row = idx >> 3, c8 = idx & 7;
        u32x4 v = *(const u32x4*)(u + (size_t)(tt * 64 + row) * D_ + dd * 64 + c8 * 8);
        const u16* e = (const u16*)&v;
#pragma unroll
        for (int j = 0; j < 8; ++j) st[(c8 * 8 + j) * 72 + row] = e[j];
      }
      SYNC();
#pragma unroll
      for (int it = 0; it < 2; ++it) {
        int idx = it * 256 + tid;
        int row = idx >> 3, c8 = idx & 7;
        u32x4 v = *(const u32x4*)(st + row * 72 + c8 * 8);
        *(u32x4*)(uT + (size_t)(dd * 64 + row) * S_ + tt * 64 + c8 * 8) = v;
      }
      SYNC();
    }
  } else {
    float gwf[16];
    {
      u32x4 w0 = *(const u32x4*)(gw + lane * 16), w1 = *(const u32x4*)(gw + lane * 16 + 8);
#pragma unroll
      for (int j = 0; j < 4; ++j) {
        gwf[2 * j] = lo16(w0[j]); gwf[2 * j + 1] = hi16(w0[j]);
        gwf[8 + 2 * j] = lo16(w1[j]); gwf[8 + 2 * j + 1] = hi16(w1[j]);
      }
    }
    for (int rr = 0; rr < 16; ++rr) {
      int row = (bid - 640) * 64 + wid * 16 + rr;
      u32x4 x0 = *(const u32x4*)(u + (size_t)row * D_ + lane * 16), x1 = *(const u32x4*)(u + (size_t)row * D_ + lane * 16 + 8);
      float s = 0.f;
#pragma unroll
      for (int j = 0; j < 4; ++j) {
        s += rbf(lo16(x0[j]) * gwf[2 * j]) + rbf(hi16(x0[j]) * gwf[2 * j + 1]);
        s += rbf(lo16(x1[j]) * gwf[8 + 2 * j]) + rbf(hi16(x1[j]) * gwf[8 + 2 * j + 1]);
      }
#pragma unroll
      for (int o = 32; o > 0; o >>= 1) s += shx(s, o);
      float x = rbf(s);
      float gg = rbf(1.f / (1.f + fexp(-x)));
      if (lane == 0) gate[row] = gg;
    }
  }
}

#define TC_LEN 2080
#define TC_BYTES (TC_LEN * 2)
#define CONV_U_OFF (8 * TC_BYTES)
#define CONV_KF_OFF (CONV_U_OFF + 4640)
#define CONV_LDS (CONV_KF_OFF + 8192 + 64)

__global__ __launch_bounds__(256, 2) NOPK void k_l2(const u16* __restrict__ uT, const float* __restrict__ Kc,
                                               const u16* __restrict__ Dskip, u16* __restrict__ aT,
                                               const u16* __restrict__ pq, const float* __restrict__ kvp,
                                               u16* __restrict__ o) {
  extern __shared__ __attribute__((aligned(16))) char smem[];
  const int bid = blockIdx.x;
  const int tid = threadIdx.x, lane = tid & 63, wid = __builtin_amdgcn_readfirstlane(tid >> 6);
  const int r16 = lane & 15, g = lane >> 4;
  if (bid < 1024) {
    const int d = bid;
    float* Kf = (float*)(smem + CONV_KF_OFF);
    u16* ul = (u16*)(smem + CONV_U_OFF);
    const float* kc = Kc + (size_t)d * KCW;
#pragma unroll
    for (int it = 0; it < 8; ++it) {
      int l = it * 256 + tid;
      int idx;
      if (l < 256) idx = l;
      else {
        int r = (l >= 1024) ? 3 : ((l >= 512) ? 2 : 1);
        int step = 1 << r, half = step >> 1;
        int base = l & ~(step - 1), rem = l & (step - 1);
        int rl = base;
        if (rem > half || (rem == half && (base & step))) rl += step;
        if (rl >= 2048) idx = 640;
        else {
          int r2 = (rl >= 1024) ? 3 : ((rl >= 512) ? 2 : 1);
          idx = 256 + (r2 - 1) * 128 + ((rl - (128 << r2)) >> r2);
        }
      }
      Kf[l] = kc[idx];
    }
    {
      u32x4 v = *(const u32x4*)(uT + (size_t)d * S_ + tid * 8);
      *(u32x4*)(ul + 272 + tid * 8) = v;
      if (tid < 34) *(u32x4*)(ul + tid * 8) = (u32x4){0u, 0u, 0u, 0u};
    }
    SYNC();
    for (int qb = tid; qb < 258; qb += 256) {
      float kx[11];
#pragma unroll
      for (int j = 0; j < 11; ++j) {
        int l = 2048 - (qb * 8 + j);
        kx[j] = (l >= 0 && l < 2048) ? Kf[l] : 0.f;
      }
      unsigned hw[11], lw[11];
#pragma unroll
      for (int j = 0; j < 11; ++j) {
        unsigned h = pk2(kx[j], 0.f) & 0xffffu;
        hw[j] = h;
        lw[j] = pk2(kx[j] - U2F(h << 16), 0.f) & 0xffffu;
      }
#pragma unroll
      for (int c = 0; c < 4; ++c) {
        u32x4 vh, vl;
#pragma unroll
        for (int j = 0; j < 4; ++j) {
          vh[j] = hw[c + 2 * j] | (hw[c + 2 * j + 1] << 16);
          vl[j] = lw[c + 2 * j] | (lw[c + 2 * j + 1] << 16);
        }
        *(u32x4*)(smem + c * TC_BYTES + qb * 16) = vh;
        *(u32x4*)(smem + (4 + c) * TC_BYTES + qb * 16) = vl;
      }
    }
    SYNC();
    f32x4 acc[8];
#pragma unroll
    for (int c = 0; c < 8; ++c) acc[c] = (f32x4){0.f, 0.f, 0.f, 0.f};
    const int cc = (-r16) & 3;
    const int i0_base = 2048 - 32 * wid - 16 * (g >> 1) + 8 * (g & 1) - r16 - cc;
    const char* abase = smem + cc * TC_BYTES + 2 * i0_base;
    const char* bbase = (const char*)ul + 2 * (272 + 16 * (r16 - (g >> 1)) + 8 * (g & 1)) - 64 * wid;
#pragma unroll
    for (int q = 0; q < 8; ++q) {
#pragma unroll
      for (int h = 0; h < 2; ++h) {
        const int pofs = 8 * q + 4 * h;
        s16x4 h0 = *(const s16x4*)(abase - 64 * pofs);
        s16x4 h1 = *(const s16x4*)(abase - 64 * pofs + 8);
        s16x4 l0 = *(const s16x4*)(abase - 64 * pofs + 4 * TC_BYTES);
        s16x4 l1 = *(const s16x4*)(abase - 64 * pofs + 4 * TC_BYTES + 8);
        bf16x8 ah = __builtin_shufflevector(h0, h1, 0, 1, 2, 3, 4, 5, 6, 7);
        bf16x8 al = __builtin_shufflevector(l0, l1, 0, 1, 2, 3, 4, 5, 6, 7);
#pragma unroll
        for (int c = q; c < 8; ++c) {
          bf16x8 bfr = *(const bf16x8*)(bbase + 512 * c - 64 * pofs);
          acc[c] = MFMA16(ah, bfr, acc[c]);
          acc[c] = MFMA16(al, bfr, acc[c]);
        }
      }
    }
    SYNC();
    float* red = (float*)smem;
#pragma unroll
    for (int c = 0; c < 8; ++c) *(f32x4*)(red + wid * 2048 + 16 * (16 * c + r16) + 4 * g) = acc[c];
    SYNC();
    {
      const float dsk = bf2f(Dskip[d]);
      f32x4 s0 = *(const f32x4*)(red + tid * 8), s1 = *(const f32x4*)(red + tid * 8 + 4);
#pragma unroll
      for (int w = 1; w < 4; ++w) {
        s0 += *(const f32x4*)(red + w * 2048 + tid * 8);
        s1 += *(const f32x4*)(red + w * 2048 + tid * 8 + 4);
      }
      u32x4 uv = *(const u32x4*)(ul + 272 + tid * 8);
      u32x4 ov;
#pragma unroll
      for (int j = 0; j < 4; ++j) {
        float y0 = j < 2 ? s0[2 * j] : s1[2 * j - 4], y1 = j < 2 ? s0[2 * j + 1] : s1[2 * j - 3];
        float a0 = rbf(y0) + rbf(lo16(uv[j]) * dsk), a1 = rbf(y1) + rbf(hi16(uv[j]) * dsk);
        ov[j] = pk2(a0, a1);
      }
      *(u32x4*)(aT + (size_t)d * S_ + tid * 8) = ov;
    }
  } else {
    const int ob = bid - 1024;
    const int h = ob >> 3, rt = ob & 7;
    u16* kvT = (u16*)smem;
    u16* zs = (u16*)(smem + 4096);
    const float* src = kvp + (size_t)h * 16 * 1056;
    for (int i = tid; i < 1056; i += 256) {
      float s = 0.f;
#pragma unroll
      for (int p = 0; p < 16; ++p) s += src[p * 1056 + i];
      u16 b = (u16)(pk2(s, 0.f) & 0xffffu);
      if (i < 1024) kvT[(i & 31) * 40 + (i >> 5)] = b;
      else zs[i - 1024] = b;
    }
    SYNC();
    bf16x8 ka0 = *(const bf16x8*)(kvT + r16 * 40 + 8 * g);
    bf16x8 ka1 = *(const bf16x8*)(kvT + (16 + r16) * 40 + 8 * g);
    bf16x8 za = *(const bf16x8*)(zs + 8 * g);
#pragma unroll
    for (int tt = 0; tt < 4; ++tt) {
      int tok = rt * 256 + wid * 64 + tt * 16 + r16;
      bf16x8 bq = *(const bf16x8*)(pq + (size_t)tok * D_ + h * 32 + 8 * g);
      f32x4 zero = (f32x4){0.f, 0.f, 0.f, 0.f};
      f32x4 n0 = MFMA16(ka0, bq, zero), n1 = MFMA16(ka1, bq, zero), dn = MFMA16(za, bq, zero);
      float den = rbf(rbf(dn[0]) + 1e-6f);
      u32x2 o0, o1;
      o0[0] = pk2(rbf(n0[0]) / den, rbf(n0[1]) / den); o0[1] = pk2(rbf(n0[2]) / den, rbf(n0[3]) / den);
      o1[0] = pk2(rbf(n1[0]) / den, rbf(n1[1]) / den); o1[1] = pk2(rbf(n1[2]) / den, rbf(n1[3]) / den);
      *(u32x2*)(o + (size_t)tok * D_ + h * 32 + 4 * g) = o0;
      *(u32x2*)(o + (size_t)tok * D_ + h * 32 + 16 + 4 * g) = o1;
    }
  }
}

struct PlainCol {
  int c0;
  DI int operator()(int cg) const { return c0 + cg * 32; }
};

__global__ __launch_bounds__(256, 2) NOPK void k_l3(const u16* __restrict__ o, const u16* __restrict__ wo,
                                               const u16* __restrict__ aT, const float* __restrict__ gate,
                                               u16* __restrict__ out) {
  extern __shared__ __attribute__((aligned(16))) char smem[];
  const int bid = blockIdx.x;
  const int tid = threadIdx.x, lane = tid & 63, wid = __builtin_amdgcn_readfirstlane(tid >> 6);
  const int xcd = bid & 7, i = bid >> 3;
  const int mt = xcd * 4 + (i & 3), nt = i >> 2;
  f32x4 acc[2][4];
  PlainCol cf{nt * 128};
  gemm_core<2, 4, 1024>(o + (size_t)mt * 64 * D_, wo, cf, smem, acc);
  const int wr = wid >> 1, wc = wid & 1, r16 = lane & 15, g = lane >> 4;
  u16* so = (u16*)smem;
#pragma unroll
  for (int m = 0; m < 2; ++m) {
    int t0 = mt * 64 + wr * 32 + m * 16 + 4 * g;
    f32x4 gg = *(const f32x4*)(gate + t0);
#pragma unroll
    for (int n = 0; n < 4; ++n) {
      int ch = nt * 128 + wc * 64 + n * 16 + r16;
      u32x2 av = *(const u32x2*)(aT + (size_t)ch * S_ + t0);
#pragma unroll
      for (int j = 0; j < 4; ++j) {
        float a = (j & 1) ? hi16(av[j >> 1]) : lo16(av[j >> 1]);
        float b = rbf(acc[m][n][j]);
        float gj = gg[j];
        float r = rbf(gj * a) + rbf(rbf(1.f - gj) * b);
        so[(wr * 32 + m * 16 + 4 * g + j) * 136 + wc * 64 + n * 16 + r16] = (u16)(pk2(r, 0.f) & 0xffffu);
      }
    }
  }
  SYNC();
#pragma unroll
  for (int it = 0; it < 4; ++it) {
    int idx = it * 256 + tid;
    int row = idx >> 4, c8 = idx & 15;
    u32x4 v = *(const u32x4*)(so + row * 136 + c8 * 8);
    *(u32x4*)(out + (size_t)(mt * 64 + row) * D_ + nt * 128 + c8 * 8) = v;
  }
}

extern "C" void kernel_launch(void* const* d_in, const int* in_sizes, int n_in, void* d_out, int out_size, void* d_ws,
                              size_t ws_size, hipStream_t stream) {
  const u16* u = (const u16*)d_in[0];
  const u16* Are = (const u16*)d_in[1];
  const u16* Aim = (const u16*)d_in[2];
  const u16* Bre = (const u16*)d_in[3];
  const u16* Bim = (const u16*)d_in[4];
  const u16* Cre = (const u16*)d_in[5];
  const u16* Cim = (const u16*)d_in[6];
  const u16* logdt = (const u16*)d_in[7];
  const u16* Dskip = (const u16*)d_in[8];
  const u16* wqkv = (const u16*)d_in[10];
  const u16* wo = (const u16*)d_in[11];
  const u16* gw = (const u16*)d_in[12];
  u16* out = (u16*)d_out;
  char* ws = (char*)d_ws;
  float* Kc = (float*)ws;
  u16* uT = (u16*)(ws + (4 << 20));
  u16* aT = (u16*)(ws + (8 << 20));
  u16* pq = (u16*)(ws + (12 << 20));
  float* kvp = (float*)(ws + (16 << 20));
  u16* o = (u16*)(ws + (20 << 20));
  float* gate = (float*)(ws + (24 << 20));
  constexpr int LDS1 = 2 * GemmCfg<4, 6>::STAGE;
  constexpr int LDS3 = 2 * GemmCfg<2, 4>::STAGE;
  hipFuncSetAttribute((const void*)k_l1, hipFuncAttributeMaxDynamicSharedMemorySize, LDS1);
  k_l1<<<672, 256, LDS1, stream>>>(u, wqkv, Are, Aim, Bre, Bim, Cre, Cim, logdt, gw, Kc, uT, pq, kvp, gate);
  k_l2<<<1280, 256, CONV_LDS, stream>>>(uT, Kc, Dskip, aT, pq, kvp, o);
  k_l3<<<256, 256, LDS3, stream>>>(o, wo, aT, gate, out);
}
```

```cpp
#include <hip/hip_runtime.h>
#include <stdint.h>

typedef unsigned short u16;
typedef __attribute__((ext_vector_type(8))) short bf16x8;
typedef __attribute__((ext_vector_type(4))) short s16x4;
typedef __attribute__((ext_vector_type(4))) float f32x4;
typedef __attribute__((ext_vector_type(2))) float f32x2;
typedef __attribute__((ext_vector_type(2))) __bf16 bf16x2_t;
typedef __attribute__((ext_vector_type(4))) unsigned u32x4;
typedef __attribute__((ext_vector_type(2))) unsigned u32x2;
typedef __attribute__((address_space(3))) s16x4* lds_s16x4_ptr;

#define NOPK __attribute__((target("no-packed-fp32-ops")))
#define DI __device__ __forceinline__ NOPK
#define U2F(x) __builtin_bit_cast(float, (unsigned)(x))
#define F2U(x) __builtin_bit_cast(unsigned, (float)(x))
#define SYNC()                                                 \
  do {                                                         \
    __builtin_amdgcn_fence(__ATOMIC_RELEASE, "workgroup");     \
    __builtin_amdgcn_s_barrier();                              \
    __builtin_amdgcn_fence(__ATOMIC_ACQUIRE, "workgroup");     \
  } while (0)
#define MFMA16(a, b, c) __builtin_amdgcn_mfma_f32_16x16x32_bf16((a), (b), (c), 0, 0, 0)

#define ST(k, i)
#define S_ 2048
#define D_ 1024
#define NST 128
#define KCW 648

DI float bf2f(u16 v) { return U2F(((unsigned)v) << 16); }
DI unsigned pk2(float lo, float hi) {
  f32x2 f = {lo, hi};
  bf16x2_t b = __builtin_convertvector(f, bf16x2_t);
  return __builtin_bit_cast(unsigned, b);
}
DI float rbf(float f) { return U2F(pk2(f, 0.f) << 16); }
DI float lo16(unsigned p) { return U2F(p << 16); }
DI float hi16(unsigned p) { return U2F(p & 0xffff0000u); }

DI float fexp(float x) {
  float t = x * 1.44269504088896340736f;
  float e = __builtin_fmaf(x, 1.44269504088896340736f, -t) + x * 1.92596299112661746e-8f;
  return __builtin_amdgcn_exp2f(t) * (1.f + e * 0.69314718056f);
}
DI float shx(float v, int m) {
  int lane = threadIdx.x & 63;
  return U2F(__builtin_amdgcn_ds_bpermute((lane ^ m) << 2, (int)F2U(v)));
}
DI float phi_f(float t) {
  float r = t > 0.f ? rbf(t + 1.f) : rbf(fexp(t));
  return rbf(r + 1e-6f);
}

DI unsigned phi_pk(unsigned p) {
  float t0 = lo16(p), t1 = hi16(p);
  float e0 = __builtin_amdgcn_exp2f(t0 * 1.44269504088896340736f), e1 = __builtin_amdgcn_exp2f(t1 * 1.44269504088896340736f);
  float r0 = t0 > 0.f ? t0 + 1.f : e0, r1 = t1 > 0.f ? t1 + 1.f : e1;
  unsigned q = pk2(r0, r1);
  return pk2(lo16(q) + 1e-6f, hi16(q) + 1e-6f);
}

DI void cexp_fast(float xr, float xi, float& o_r, float& o_i) {
  float e = __builtin_amdgcn_exp2f(xr * 1.44269504088896340736f);
  float rev = xi * 0.15915494309189533577f;
  rev = __builtin_amdgcn_fractf(rev);
  o_r = e * __builtin_amdgcn_cosf(rev);
  o_i = e * __builtin_amdgcn_sinf(rev);
}

DI void split8(const float* x, bf16x8& hi, bf16x8& lo) {
  u32x4 h, l;
#pragma unroll
  for (int i = 0; i < 4; ++i) {
    unsigned ph = pk2(x[2 * i], x[2 * i + 1]);
    h[i] = ph;
    l[i] = pk2(x[2 * i] - lo16(ph), x[2 * i + 1] - hi16(ph));
  }
  hi = __builtin_bit_cast(bf16x8, h);
  lo = __builtin_bit_cast(bf16x8, l);
}

DI int krow_l0(int tile, int row) {
  if (tile == 0) return (row & 7) + 128 * (row >> 3);
  if (tile == 1) return row < 8 ? 256 + 2 * (row & 3) + 128 * (row >> 2) : 512 + 4 * (row & 1) + 128 * ((row - 8) >> 1);
  return row < 8 ? 1024 + 128 * row : (row == 8 ? 2048 : 0);
}
DI int kmap(int l) {
  int rl = (int)rbf((float)l);
  int b = (rl >> 3) & 15, hi = (rl >> 7) & 7;
  int idx;
  if (rl < 256) idx = ((rl & 7) + 8 * hi) * 16 + b;
  else if (rl < 512) idx = 256 + (((rl >> 1) & 3) + 4 * (hi & 1)) * 16 + b;
  else if (rl < 1024) idx = 256 + (8 + ((rl >> 2) & 1) + 2 * (hi & 3)) * 16 + b;
  else if (rl < 2048) idx = 512 + hi * 16 + b;
  else idx = 512 + 8 * 16;
  return idx;
}
struct KgenState {
  const u16 *Are, *Aim, *Bre, *Bim, *Cre, *Cim;
  float* Kc;
  int d;
  float dt, fl[3], fb;
  unsigned pv[12], pn[12];
  float xb[8], xa[3][8];
  f32x4 acc[3];
};
DI void kg_init(KgenState& k, int lane, const u16* logdt) {
  k.dt = rbf(fexp(bf2f(logdt[k.d])));
  const int a = lane & 15;
#pragma unroll
  for (int r = 0; r < 3; ++r) { k.fl[r] = (float)krow_l0(r, a); k.acc[r] = (f32x4){0.f, 0.f, 0.f, 0.f}; }
  k.fb = (float)(8 * a);
}
DI void kg_load1(unsigned& o0, unsigned& o1, const u16* arr, int off, int g) {
  const unsigned* p = (const unsigned*)(arr + off);
  unsigned v0 = __builtin_amdgcn_readfirstlane(p[0]), v1 = __builtin_amdgcn_readfirstlane(p[1]);
  unsigned v2 = __builtin_amdgcn_readfirstlane(p[2]), v3 = __builtin_amdgcn_readfirstlane(p[3]);
  unsigned v4 = __builtin_amdgcn_readfirstlane(p[4]), v5 = __builtin_amdgcn_readfirstlane(p[5]);
  unsigned v6 = __builtin_amdgcn_readfirstlane(p[6]), v7 = __builtin_amdgcn_readfirstlane(p[7]);
  unsigned x0 = (g & 1) ? v2 : v0, y0 = (g & 1) ? v6 : v4;
  unsigned x1 = (g & 1) ? v3 : v1, y1 = (g & 1) ? v7 : v5;
  o0 = (g & 2) ? y0 : x0;
  o1 = (g & 2) ? y1 : x1;
}
DI void kg_load(KgenState& k, int ks, int g) {
  const int off = k.d * NST + 16 * ks;
  kg_load1(k.pv[0], k.pv[1], k.Are, off, g);
  kg_load1(k.pv[2], k.pv[3], k.Aim, off, g);
  kg_load1(k.pv[4], k.pv[5], k.Bre, off, g);
  kg_load1(k.pv[6], k.pv[7], k.Bim, off, g);
  kg_load1(k.pv[8], k.pv[9], k.Cre, off, g);
  kg_load1(k.pv[10], k.pv[11], k.Cim, off, g);
}
DI void kg_loadv(KgenState& k, int ks, int g) {
  const int off = k.d * NST + 16 * ks + 4 * g;
  u32x2 t;
  t = *(const u32x2*)(k.Are + off); k.pn[0] = t[0]; k.pn[1] = t[1];
  t = *(const u32x2*)(k.Aim + off); k.pn[2] = t[0]; k.pn[3] = t[1];
  t = *(const u32x2*)(k.Bre + off); k.pn[4] = t[0]; k.pn[5] = t[1];
  t = *(const u32x2*)(k.Bim + off); k.pn[6] = t[0]; k.pn[7] = t[1];
  t = *(const u32x2*)(k.Cre + off); k.pn[8] = t[0]; k.pn[9] = t[1];
  t = *(const u32x2*)(k.Cim + off); k.pn[10] = t[0]; k.pn[11] = t[1];
}
DI void cexp_pre(float xr2, float xi2, float& o_r, float& o_i) {
  float e = __builtin_amdgcn_exp2f(xr2);
  float rev = __builtin_amdgcn_fractf(xi2);
  o_r = e * __builtin_amdgcn_cosf(rev);
  o_i = e * __builtin_amdgcn_sinf(rev);
}
#define MFMA4F(a, b, c) __builtin_amdgcn_mfma_f32_16x16x4f32((a), (b), (c), 0, 0, 0)
template <int I>
DI void kg_quarter(KgenState& k) {
#define KEX(t) ((I & 1) ? hi16(k.pv[2 * (t) + (I >> 1)]) : lo16(k.pv[2 * (t) + (I >> 1)]))
  const float ar = k.dt * KEX(0), ai = k.dt * KEX(1), br = k.dt * KEX(2), bi = k.dt * KEX(3), c_r = KEX(4), c_i = KEX(5);
#undef KEX
  const float cr = c_r * br - c_i * bi, ci = c_r * bi + c_i * br;
  const float ar2 = ar * 1.44269504088896340736f, ai2 = ai * 0.15915494309189533577f;
  float pr, pi;
  cexp_pre(ar2 * k.fb, ai2 * k.fb, pr, pi);
  k.xb[2 * I] = cr * pr - ci * pi;
  k.xb[2 * I + 1] = -(cr * pi + ci * pr);
#pragma unroll
  for (int r = 0; r < 3; ++r) cexp_pre(ar2 * k.fl[r], ai2 * k.fl[r], k.xa[r][2 * I], k.xa[r][2 * I + 1]);
}
DI void kg_finish(KgenState& k) {
  bf16x8 bh, bl;
  split8(k.xb, bh, bl);
#pragma unroll
  for (int r = 0; r < 3; ++r) {
    bf16x8 ah, al;
    split8(k.xa[r], ah, al);
    k.acc[r] = MFMA16(ah, bh, k.acc[r]);
    k.acc[r] = MFMA16(ah, bl, k.acc[r]);
    k.acc[r] = MFMA16(al, bh, k.acc[r]);
  }
}
DI void kg_store(KgenState& k, int lane) {
  float* out = k.Kc + (size_t)k.d * 768;
#pragma unroll
  for (int r = 0; r < 3; ++r)
#pragma unroll
    for (int j = 0; j < 4; ++j) out[r * 256 + 16 * (4 * (lane >> 4) + j) + (lane & 15)] = k.acc[r][j];
}
struct NoSide {
  static constexpr bool kHas = false;
  DI void begin(int) {}
  DI void even(int, int) {}
  DI void odd() {}
  DI void end(int, int) {}
};
struct L1Side {
  static constexpr bool kHas = true;
  KgenState k;
  const u16 *logdt, *u, *gw;
  float* gate;
  int row0;
  DI void begin(int lane) { kg_loadv(k, 0, lane >> 4); kg_init(k, lane, logdt); }
  DI void even(int ks, int lane) {
#pragma unroll
    for (int j = 0; j < 12; ++j) k.pv[j] = k.pn[j];
    kg_loadv(k, (ks + 1) & 7, lane >> 4);
    kg_quarter<0>(k); kg_quarter<1>(k);
  }
  DI void odd() { kg_quarter<2>(k); kg_quarter<3>(k); kg_finish(k); }
  DI void end(int wid8, int lane) {
    kg_store(k, lane);
    u32x4 w0 = *(const u32x4*)(gw + lane * 16), w1 = *(const u32x4*)(gw + lane * 16 + 8);
#pragma unroll
    for (int rr = 0; rr < 2; ++rr) {
      const int row = row0 + (wid8 - 8) * 2 + rr;
      u32x4 x0 = *(const u32x4*)(u + (size_t)row * D_ + lane * 16), x1 = *(const u32x4*)(u + (size_t)row * D_ + lane * 16 + 8);
      float s = 0.f;
#pragma unroll
      for (int j = 0; j < 4; ++j) {
        s += rbf(lo16(x0[j]) * lo16(w0[j])) + rbf(hi16(x0[j]) * hi16(w0[j]));
        s += rbf(lo16(x1[j]) * lo16(w1[j])) + rbf(hi16(x1[j]) * hi16(w1[j]));
      }
#pragma unroll
      for (int oo = 32; oo > 0; oo >>= 1) s += shx(s, oo);
      float x = rbf(s);
      float gg = rbf(1.f / (1.f + fexp(-x)));
      if (lane == 0) gate[row] = gg;
    }
  }
};

template <int MT, int NT, int NS, int UPS>
struct GemmCfg {
  static constexpr int BM = 32 * MT;
  static constexpr int NCG = NT;
  static constexpr int A_BYTES = BM * 128;
  static constexpr int B_BYTES = NCG * 8 * 512;
  static constexpr int UNIT = A_BYTES + B_BYTES;
  static constexpr int STAGE = UPS * UNIT;
  static constexpr int NSTG = 16 / UPS;
  static constexpr int G = UPS * (MT + NT);
  static constexpr int LDS = NS * STAGE;
};

struct NoHook {
  DI void run(int, const char*) const {}
};

template <int MT, int NT, int NS, int UPS, int LDB>
struct GemmOps {
  using C = GemmCfg<MT, NT, NS, UPS>;
  static constexpr int NCG = C::NCG;
  DI static bf16x8 reada(const char* sb, int j, const unsigned (&aoff)[2], int m) {
    return *(const bf16x8*)(sb + (j >> 1) * C::UNIT + aoff[j & 1] + m * 2048);
  }
  DI static bf16x8 readb(const char* sb, int j, const unsigned (&boff)[2][2], int n) {
    const char* p = sb + (j >> 1) * C::UNIT + (4 * (j & 1) * NCG + (n >> 1)) * 512;
    s16x4 t0 = __builtin_amdgcn_ds_read_tr16_b64_v4i16((lds_s16x4_ptr)(p + boff[n & 1][0]));
    s16x4 t1 = __builtin_amdgcn_ds_read_tr16_b64_v4i16((lds_s16x4_ptr)(p + boff[n & 1][1]));
    return __builtin_shufflevector(t0, t1, 0, 1, 2, 3, 4, 5, 6, 7);
  }
  DI static void issue(char* sbase, int ku0, int wid, int lane, const u16* const (&asrc)[MT], const u16* const (&bsrc)[NT]) {
#pragma unroll
    for (int uu = 0; uu < UPS; ++uu) {
      const int ku = (ku0 + uu) & 15;
      char* sa = sbase + uu * C::UNIT;
      char* sb = sa + C::A_BYTES;
#pragma unroll
      for (int i = 0; i < MT; ++i)
        __builtin_amdgcn_global_load_lds((const unsigned*)(asrc[i] + ku * 64), (unsigned*)(sa + (wid * MT + i) * 1024 + lane * 16), 16, 0, 0);
#pragma unroll
      for (int i = 0; i < NT; ++i)
        __builtin_amdgcn_global_load_lds((const unsigned*)(bsrc[i] + (size_t)ku * 64 * LDB), (unsigned*)(sb + (wid * NT + i) * 1024 + lane * 16), 16, 0, 0);
    }
  }
};

template <int MT, int NT, int NS, int UPS, int LDB, typename ColFn, typename Hook, typename Side>
DI bool gemm_core(const u16* __restrict__ A, const u16* __restrict__ B, ColFn colfn, char* smem, f32x4 (&acc)[MT][NT],
                  const Hook& hook, int rot, Side& side) {
  using C = GemmCfg<MT, NT, NS, UPS>;
  using O = GemmOps<MT, NT, NS, UPS, LDB>;
  constexpr int KID = (MT == 4) ? 0 : 2;
  constexpr int NCG = C::NCG;
  constexpr int NSTG = C::NSTG;
  const int tid = threadIdx.x, lane = tid & 63, wid8 = __builtin_amdgcn_readfirstlane(tid >> 6);
  const int wid = wid8 & 3;
  if (Side::kHas && wid8 >= 8) {
    side.begin(lane);
#pragma unroll 1
    for (int ks = 0; ks < NSTG / 2; ++ks) {
      side.even(ks, lane);
      asm volatile("s_barrier" ::: "memory");
      side.odd();
      asm volatile("s_barrier" ::: "memory");
    }
    asm volatile("s_barrier" ::: "memory");
    side.end(wid8, lane);
    return false;
  }
  if (wid8 >= 4) {
    const u16* asrc[MT];
#pragma unroll
    for (int i = 0; i < MT; ++i) {
      int P = (wid * MT + i) * 64 + lane;
      int line = P >> 4, slot = P & 15;
      int row = 2 * line + (slot >> 3), chunk = (slot & 7) ^ (line & 7);
      asrc[i] = A + (size_t)row * D_ + chunk * 8;
    }
    const u16* bsrc[NT];
#pragma unroll
    for (int i = 0; i < NT; ++i) {
      int ins = wid * NT + i;
      int kb = ins / (NCG / 2), cgp = ins % (NCG / 2);
      int sub = lane >> 5, r7 = (lane & 31) >> 2, chp = lane & 3;
      int row = kb * 8 + r7;
      int ch = chp ^ ((row >> 2) & 3);
      bsrc[i] = B + (size_t)row * LDB + colfn(cgp * 2 + sub) + ch * 8;
    }
#pragma unroll 1
    for (int st = 0; st < NS; ++st) O::issue(smem + st * C::STAGE, st * UPS + rot, wid, lane, asrc, bsrc);
    asm volatile("s_waitcnt vmcnt(%0)\n\ts_barrier" ::"i"(C::G * (NS - 1)) : "memory");
    int buf = 0;
#pragma unroll 1
    for (int s = 0; s + NS < NSTG; ++s) {
      asm volatile("s_waitcnt vmcnt(%0)\n\ts_barrier" ::"i"(C::G * (NS - 2)) : "memory");
      O::issue(smem + buf * C::STAGE, (s + NS) * UPS + rot, wid, lane, asrc, bsrc);
      buf = (buf + 1 == NS) ? 0 : buf + 1;
    }
    if constexpr (NS >= 4) asm volatile("s_waitcnt vmcnt(%0)\n\ts_barrier" ::"i"(C::G * 2) : "memory");
    if constexpr (NS >= 3) asm volatile("s_waitcnt vmcnt(%0)\n\ts_barrier" ::"i"(C::G * 1) : "memory");
    asm volatile("s_waitcnt vmcnt(0)\n\ts_barrier" ::: "memory");
    asm volatile("s_barrier" ::: "memory");
    return false;
  }
  const int wr = wid >> 1, wc = wid & 1;
  const int r16 = lane & 15, g = lane >> 4;
  unsigned aoff[2];
  {
    int row = wr * (16 * MT) + r16;
#pragma unroll
    for (int ks = 0; ks < 2; ++ks) aoff[ks] = (row >> 1) * 256 + (((row & 1) * 8) + ((4 * ks + g) ^ ((row >> 1) & 7))) * 16;
  }
  unsigned boff[2][2];
  {
    int q = r16 >> 2, p = lane & 3;
#pragma unroll
    for (int half = 0; half < 2; ++half)
#pragma unroll
      for (int t = 0; t < 2; ++t)
        boff[half][t] = C::A_BYTES + (g * NCG + wc * (NT / 2)) * 512 + (4 * t + q) * 64 +
                        ((2 * (half ^ (g & 1))) + ((p >> 1) ^ t)) * 16 + 8 * (p & 1);
  }
#pragma unroll
  for (int m = 0; m < MT; ++m)
#pragma unroll
    for (int n = 0; n < NT; ++n) acc[m][n] = (f32x4){0.f, 0.f, 0.f, 0.f};
  ST(KID, 0);
  ST(KID, 1);
  bf16x8 af[2][MT], bfr[NT];
  asm volatile("s_barrier" ::: "memory");
  ST(KID, 2);
#pragma unroll
  for (int m = 0; m < MT; ++m) af[0][m] = O::reada(smem, 0, aoff, m);
#pragma unroll
  for (int n = 0; n < NT; ++n) bfr[n] = O::readb(smem, 0, boff, n);
  int buf = 0;
#pragma unroll 1
  for (int s = 0; s < NSTG; ++s) {
    const char* sb = smem + buf * C::STAGE;
    const int nbuf = (buf + 1 == NS) ? 0 : buf + 1;
    const char* sn = smem + nbuf * C::STAGE;
    const bool more = s + 1 < NSTG;
#pragma unroll
    for (int uu = 0; uu < UPS; ++uu) hook.run((s * UPS + uu + rot) & 15, sb + uu * C::UNIT);
#pragma unroll
    for (int j = 0; j < 2 * UPS; ++j) {
      constexpr int LAST = 2 * UPS - 1;
      if (j < LAST) {
#pragma unroll
        for (int m = 0; m < MT; ++m) af[(j + 1) & 1][m] = O::reada(sb, j + 1, aoff, m);
#pragma unroll
        for (int n = 0; n < NT; ++n) {
#pragma unroll
          for (int m = 0; m < MT; ++m) acc[m][n] = MFMA16(af[j & 1][m], bfr[n], acc[m][n]);
          bfr[n] = O::readb(sb, j + 1, boff, n);
        }
      } else {
        asm volatile("s_waitcnt lgkmcnt(0)\n\ts_barrier" ::: "memory");
#pragma unroll
        for (int m = 0; m < MT; ++m) af[0][m] = O::reada(sn, 0, aoff, m);
#pragma unroll
        for (int n = 0; n < NT; ++n) {
#pragma unroll
          for (int m = 0; m < MT; ++m) acc[m][n] = MFMA16(af[1][m], bfr[n], acc[m][n]);
          bfr[n] = O::readb(sn, 0, boff, n);
        }
      }
    }
    (void)more;
    buf = nbuf;
  }
  asm volatile("s_waitcnt lgkmcnt(0)" ::: "memory");
  ST(KID, 3);
  return true;
}

struct QkvCol {
  int hp;
  DI int operator()(int cg) const { return (cg % 3) * 1024 + hp * 64 + (cg / 3) * 32; }
};
struct TrHook {
  int hp, mt;
  u16* uT;
  DI void run(int ku, const char* sbuf) const {
    if (ku == hp) {
      const int tid = threadIdx.x;
      const int c = tid & 63, tg = tid >> 6;
      u16* dst = uT + (size_t)(ku * 64 + c) * S_ + mt * 128 + tg * 32;
#pragma unroll 1
      for (int hh = 0; hh < 4; ++hh) {
        unsigned w[4];
#pragma unroll
        for (int j = 0; j < 4; ++j) {
          int r0 = tg * 32 + 8 * hh + 2 * j;
          const char* p = sbuf + (r0 >> 1) * 256 + (((c >> 3) ^ ((r0 >> 1) & 7)) * 16) + (c & 7) * 2;
          unsigned a0 = *(const u16*)p, a1 = *(const u16*)(p + 128);
          w[j] = a0 | (a1 << 16);
        }
        *(u32x4*)(dst + 8 * hh) = (u32x4){w[0], w[1], w[2], w[3]};
      }
    }
  }
};

#define NS1 4
__global__ __launch_bounds__(768, 3) NOPK void k_l1(const u16* __restrict__ u, const u16* __restrict__ wqkv,
                                                    const u16* __restrict__ Are, const u16* __restrict__ Aim,
                                                    const u16* __restrict__ Bre, const u16* __restrict__ Bim,
                                                    const u16* __restrict__ Cre, const u16* __restrict__ Cim,
                                                    const u16* __restrict__ logdt, const u16* __restrict__ gw,
                                                    float* __restrict__ Kc, float* __restrict__ gate, u16* __restrict__ uT, u16* __restrict__ pq, float* __restrict__ kvp) {
  extern __shared__ __attribute__((aligned(16))) char smem[];
  const int bid = blockIdx.x;
  const int tid = threadIdx.x, lane = tid & 63, wid = __builtin_amdgcn_readfirstlane(tid >> 6);
  const int xcd = bid & 7, i = bid >> 3;
  const int mt = (xcd & 3) * 4 + (i & 3), hp = (xcd >> 2) * 8 + (i >> 2);
  f32x4 acc[4][6];
  QkvCol cf{hp};
  const int rot = i & 15;
  TrHook hook{hp, mt, uT};
  L1Side side;
  side.k.Are = Are; side.k.Aim = Aim; side.k.Bre = Bre; side.k.Bim = Bim; side.k.Cre = Cre; side.k.Cim = Cim;
  side.k.Kc = Kc; side.k.d = bid * 4 + (wid & 3); side.logdt = logdt;
  side.u = u; side.gw = gw; side.gate = gate; side.row0 = mt * 128 + hp * 8;
  if (!gemm_core<4, 6, NS1, 1, 3072>(u + (size_t)mt * 128 * D_, wqkv, cf, smem, acc, hook, rot, side)) return;
  const int wr = (wid >> 1) & 1, wc = wid & 1, r16 = lane & 15, g = lane >> 4;
  u16* sq = (u16*)smem;
#pragma unroll
  for (int m = 0; m < 4; ++m)
#pragma unroll
    for (int n = 0; n < 2; ++n) {
      unsigned p0 = pk2(acc[m][n][0], acc[m][n][1]), p1 = pk2(acc[m][n][2], acc[m][n][3]);
      u16* dst = sq + (wr * 64 + m * 16 + 4 * g) * 72 + wc * 32 + n * 16 + r16;
      dst[0] = (u16)p0; dst[72] = (u16)(p0 >> 16); dst[144] = (u16)p1; dst[216] = (u16)(p1 >> 16);
    }
  f32x4 kvacc[2][2], zacc[2];
#pragma unroll
  for (int x = 0; x < 2; ++x) {
    zacc[x] = (f32x4){0.f, 0.f, 0.f, 0.f};
#pragma unroll
    for (int y = 0; y < 2; ++y) kvacc[x][y] = (f32x4){0.f, 0.f, 0.f, 0.f};
  }
  bf16x8 ones;
#pragma unroll
  for (int j = 0; j < 8; ++j) ones[j] = (short)0x3f80;
#pragma unroll
  for (int mp = 0; mp < 2; ++mp) {
    bf16x8 kf[2], vf[2];
#pragma unroll
    for (int x = 0; x < 2; ++x) {
      u32x4 pk_, pv_;
#pragma unroll
      for (int h = 0; h < 2; ++h) {
        const f32x4 ka = acc[2 * mp + h][2 + x], va = acc[2 * mp + h][4 + x];
        pk_[2 * h] = phi_pk(pk2(ka[0], ka[1]));
        pk_[2 * h + 1] = phi_pk(pk2(ka[2], ka[3]));
        pv_[2 * h] = pk2(va[0], va[1]);
        pv_[2 * h + 1] = pk2(va[2], va[3]);
      }
      kf[x] = __builtin_bit_cast(bf16x8, pk_);
      vf[x] = __builtin_bit_cast(bf16x8, pv_);
    }
#pragma unroll
    for (int x = 0; x < 2; ++x) {
#pragma unroll
      for (int y = 0; y < 2; ++y) kvacc[x][y] = MFMA16(kf[x], vf[y], kvacc[x][y]);
      zacc[x] = MFMA16(kf[x], ones, zacc[x]);
    }
  }
  float* sred = (float*)(smem + 20480) + wc * 1056;
  if (wr == 1) {
#pragma unroll
    for (int x = 0; x < 2; ++x) {
#pragma unroll
      for (int y = 0; y < 2; ++y)
#pragma unroll
        for (int j = 0; j < 4; ++j) sred[(x * 16 + 4 * g + j) * 32 + y * 16 + r16] = kvacc[x][y][j];
      if (r16 == 0)
#pragma unroll
        for (int j = 0; j < 4; ++j) sred[1024 + x * 16 + 4 * g + j] = zacc[x][j];
    }
  }
  SYNC();
  if (wr == 0) {
    float* dst = kvp + ((size_t)(hp * 2 + wc) * 16 + mt) * 1056;
#pragma unroll
    for (int x = 0; x < 2; ++x) {
#pragma unroll
      for (int y = 0; y < 2; ++y)
#pragma unroll
        for (int j = 0; j < 4; ++j) {
          int idx = (x * 16 + 4 * g + j) * 32 + y * 16 + r16;
          dst[idx] = kvacc[x][y][j] + sred[idx];
        }
      if (r16 == 0)
#pragma unroll
        for (int j = 0; j < 4; ++j) {
          int idx = 1024 + x * 16 + 4 * g + j;
          dst[idx] = zacc[x][j] + sred[idx];
        }
    }
  }
#pragma unroll
  for (int it = 0; it < 4; ++it) {
    int idx = it * 256 + tid;
    int row = idx >> 3, c8 = idx & 7;
    u32x4 v = *(const u32x4*)(sq + row * 72 + c8 * 8);
    *(u32x4*)(pq + (size_t)(mt * 128 + row) * D_ + hp * 64 + c8 * 8) = v;
  }
  ST(0, 4);
  asm volatile("s_waitcnt vmcnt(0)" ::: "memory");
  ST(0, 5);
}

#define TC_LEN 2080
#define TC_BYTES (TC_LEN * 2)
#define CONV_U_OFF (4 * TC_BYTES)
#define CONV_KC_OFF (CONV_U_OFF + 4640)
#define CONV_LDS (CONV_KC_OFF + 3072)

__global__ __launch_bounds__(256, 4) NOPK void k_l2(const u16* __restrict__ uT,
                                                    const float* __restrict__ Kg, const u16* __restrict__ Dskip,
                                                    u16* __restrict__ aT,
                                                    const u16* __restrict__ pq, const float* __restrict__ kvp,
                                                    u16* __restrict__ o) {
  extern __shared__ __attribute__((aligned(16))) char smem[];
  const int bid = blockIdx.x;
  const int tid = threadIdx.x, lane = tid & 63, wid = __builtin_amdgcn_readfirstlane(tid >> 6);
  const int r16 = lane & 15, g = lane >> 4;
  ST(1, 0);
  if (bid < 256) {
    const int h = bid >> 3, rt = bid & 7;
    u16* kvT = (u16*)smem;
    u16* zs = (u16*)(smem + 4096);
    u32x4 bqv[4];
#pragma unroll
    for (int tt = 0; tt < 4; ++tt)
      bqv[tt] = *(const u32x4*)(pq + (size_t)(rt * 256 + wid * 64 + tt * 16 + r16) * D_ + h * 32 + 8 * g);
    const float* src = kvp + (size_t)h * 16 * 1056;
    {
      f32x4 pv4[16];
      f32x4 zq[16];
#pragma unroll
      for (int p = 0; p < 16; ++p) pv4[p] = *(const f32x4*)(src + p * 1056 + tid * 4);
#pragma unroll
      for (int p = 0; p < 16; ++p) zq[p] = *(const f32x4*)(src + p * 1056 + 1024 + (tid & 7) * 4);
      f32x4 zv = zq[0];
#pragma unroll
      for (int p = 1; p < 16; ++p) zv += zq[p];
      f32x4 sv = pv4[0];
#pragma unroll
      for (int p = 1; p < 16; ++p) sv += pv4[p];
      const int dd = tid >> 3, e0 = (tid & 7) * 4;
#pragma unroll
      for (int j = 0; j < 4; ++j) kvT[(e0 + j) * 40 + dd] = (u16)(pk2(sv[j], 0.f) & 0xffffu);
      if (tid < 8) {
        u32x2 zz; zz[0] = pk2(zv[0], zv[1]); zz[1] = pk2(zv[2], zv[3]);
        *(u32x2*)(zs + tid * 4) = zz;
      }
    }
    SYNC();
    bf16x8 ka0 = *(const bf16x8*)(kvT + r16 * 40 + 8 * g);
    bf16x8 ka1 = *(const bf16x8*)(kvT + (16 + r16) * 40 + 8 * g);
    bf16x8 za = *(const bf16x8*)(zs + 8 * g);
#pragma unroll
    for (int tt = 0; tt < 4; ++tt) {
      int tok = rt * 256 + wid * 64 + tt * 16 + r16;
      u32x4 pv;
#pragma unroll
      for (int j = 0; j < 4; ++j) pv[j] = phi_pk(bqv[tt][j]);
      bf16x8 bq = __builtin_bit_cast(bf16x8, pv);
      f32x4 zero = (f32x4){0.f, 0.f, 0.f, 0.f};
      f32x4 n0 = MFMA16(ka0, bq, zero), n1 = MFMA16(ka1, bq, zero), dn = MFMA16(za, bq, zero);
      float den = rbf(rbf(dn[0]) + 1e-6f);
      u32x2 o0, o1;
      o0[0] = pk2(rbf(n0[0]) / den, rbf(n0[1]) / den); o0[1] = pk2(rbf(n0[2]) / den, rbf(n0[3]) / den);
      o1[0] = pk2(rbf(n1[0]) / den, rbf(n1[1]) / den); o1[1] = pk2(rbf(n1[2]) / den, rbf(n1[3]) / den);
      *(u32x2*)(o + (size_t)tok * D_ + h * 32 + 4 * g) = o0;
      *(u32x2*)(o + (size_t)tok * D_ + h * 32 + 16 + 4 * g) = o1;
    }
    SYNC();
  }
  ST(1, 1);
  {
    const int d = bid;
    float* Kc = (float*)(smem + CONV_KC_OFF);
    u16* ul = (u16*)(smem + CONV_U_OFF);
    u32x4 uv = *(const u32x4*)(uT + (size_t)d * S_ + tid * 8);
    if (tid < 192) *(f32x4*)(Kc + tid * 4) = *(const f32x4*)(Kg + (size_t)d * 768 + tid * 4);
    *(u32x4*)(ul + 272 + tid * 8) = uv;
    if (tid < 34) *(u32x4*)(ul + tid * 8) = (u32x4){0u, 0u, 0u, 0u};
    SYNC();
    ST(1, 2);
    {
      const int qb = tid;
      unsigned hw[9], lw[9];
#pragma unroll
      for (int j = 0; j < 9; ++j) {
        int l = 2048 - (qb * 8 + j);
        float kx = (l < 2048) ? Kc[kmap(l)] : 0.f;
        unsigned h = pk2(kx, 0.f) & 0xffffu;
        hw[j] = h;
        lw[j] = pk2(kx - U2F(h << 16), 0.f) & 0xffffu;
      }
#pragma unroll
      for (int c = 0; c < 2; ++c) {
        u32x4 vh, vl;
#pragma unroll
        for (int j = 0; j < 4; ++j) {
          vh[j] = hw[c + 2 * j] | (hw[c + 2 * j + 1] << 16);
          vl[j] = lw[c + 2 * j] | (lw[c + 2 * j + 1] << 16);
        }
        *(u32x4*)(smem + c * TC_BYTES + qb * 16) = vh;
        *(u32x4*)(smem + (2 + c) * TC_BYTES + qb * 16) = vl;
      }
      if (tid == 255) {
#pragma unroll
        for (int c = 0; c < 2; ++c) {
          *(u32x4*)(smem + c * TC_BYTES + 4096) = (u32x4){c == 0 ? hw[8] : 0u, 0u, 0u, 0u};
          *(u32x4*)(smem + c * TC_BYTES + 4112) = (u32x4){0u, 0u, 0u, 0u};
          *(u32x4*)(smem + (2 + c) * TC_BYTES + 4096) = (u32x4){c == 0 ? lw[8] : 0u, 0u, 0u, 0u};
          *(u32x4*)(smem + (2 + c) * TC_BYTES + 4112) = (u32x4){0u, 0u, 0u, 0u};
        }
      }
    }
    SYNC();
    ST(1, 3);
    f32x4 acc[8];
#pragma unroll
    for (int c = 0; c < 8; ++c) acc[c] = (f32x4){0.f, 0.f, 0.f, 0.f};
    const int cc = r16 & 1;
    const int i0_base = 2048 - 32 * wid - 16 * (g >> 1) + 8 * (g & 1) - r16 - cc;
    const char* abase = smem + cc * TC_BYTES + 2 * i0_base;
    const char* bbase = (const char*)ul + 2 * (272 + 16 * (r16 - (g >> 1)) + 8 * (g & 1)) - 64 * wid;
#pragma unroll
    for (int q = 0; q < 8; ++q) {
#pragma unroll
      for (int h = 0; h < 2; ++h) {
        const int pofs = 8 * q + 4 * h;
        u32x4 vh, vl;
#pragma unroll
        for (int j = 0; j < 4; ++j) {
          vh[j] = *(const unsigned*)(abase - 64 * pofs + 4 * j);
          vl[j] = *(const unsigned*)(abase - 64 * pofs + 2 * TC_BYTES + 4 * j);
        }
        bf16x8 ah = __builtin_bit_cast(bf16x8, vh), al = __builtin_bit_cast(bf16x8, vl);
#pragma unroll
        for (int c = q; c < 8; ++c) {
          bf16x8 bfr = *(const bf16x8*)(bbase + 512 * c - 64 * pofs);
          acc[c] = MFMA16(ah, bfr, acc[c]);
          acc[c] = MFMA16(al, bfr, acc[c]);
        }
      }
    }
    ST(1, 4);
    SYNC();
    float* red = (float*)smem;
    if (wid >= 2) {
#pragma unroll
      for (int c = 0; c < 8; ++c) *(f32x4*)(red + (wid - 2) * 2048 + 16 * (16 * c + r16) + 4 * g) = acc[c];
    }
    SYNC();
    if (wid < 2) {
#pragma unroll
      for (int c = 0; c < 8; ++c) {
        float* p = red + wid * 2048 + 16 * (16 * c + r16) + 4 * g;
        *(f32x4*)p = *(const f32x4*)p + acc[c];
      }
    }
    SYNC();
    {
      const float dsk = bf2f(Dskip[d]);
      f32x4 s0 = *(const f32x4*)(red + tid * 8) + *(const f32x4*)(red + 2048 + tid * 8);
      f32x4 s1 = *(const f32x4*)(red + tid * 8 + 4) + *(const f32x4*)(red + 2048 + tid * 8 + 4);
      u32x4 ov;
#pragma unroll
      for (int j = 0; j < 4; ++j) {
        float y0 = j < 2 ? s0[2 * j] : s1[2 * j - 4], y1 = j < 2 ? s0[2 * j + 1] : s1[2 * j - 3];
        float a0 = rbf(y0) + rbf(lo16(uv[j]) * dsk), a1 = rbf(y1) + rbf(hi16(uv[j]) * dsk);
        ov[j] = pk2(a0, a1);
      }
      *(u32x4*)(aT + (size_t)d * S_ + tid * 8) = ov;
    }
    ST(1, 5);
    asm volatile("s_waitcnt vmcnt(0)" ::: "memory");
    ST(1, 6);
  }
}

struct PlainCol {
  int c0;
  DI int operator()(int cg) const { return c0 + cg * 32; }
};
#define NS3 3
__global__ __launch_bounds__(512, 2) NOPK void k_l3(const u16* __restrict__ o, const u16* __restrict__ wo,
                                                    const u16* __restrict__ aT, const float* __restrict__ gate,
                                                    u16* __restrict__ out) {
  extern __shared__ __attribute__((aligned(16))) char smem[];
  const int bid = blockIdx.x;
  const int tid = threadIdx.x, lane = tid & 63, wid = __builtin_amdgcn_readfirstlane(tid >> 6);
  const int xcd = bid & 7, i = bid >> 3;
  const int mt = xcd * 4 + (i & 3), nt = i >> 2;
  const int wr = (wid >> 1) & 1, wc = wid & 1, r16 = lane & 15, g = lane >> 4;
  f32x4 acc[2][4];
  PlainCol cf{nt * 128};
  NoHook hook;
  NoSide noside;
  u32x2 av[2][4];
  f32x4 gg[2];
  if (wid < 4) {
#pragma unroll
    for (int m = 0; m < 2; ++m) {
      int t0 = mt * 64 + wr * 32 + m * 16 + 4 * g;
      gg[m] = *(const f32x4*)(gate + t0);
#pragma unroll
      for (int n = 0; n < 4; ++n) av[m][n] = *(const u32x2*)(aT + (size_t)(nt * 128 + wc * 64 + n * 16 + r16) * S_ + t0);
    }
  }
  if (!gemm_core<2, 4, NS3, 2, 1024>(o + (size_t)mt * 64 * D_, wo, cf, smem, acc, hook, (i & 7) * 2, noside)) return;
  u16* so = (u16*)smem;
#pragma unroll
  for (int m = 0; m < 2; ++m) {
#pragma unroll
    for (int n = 0; n < 4; ++n) {
#pragma unroll
      for (int j = 0; j < 4; ++j) {
        float a = (j & 1) ? hi16(av[m][n][j >> 1]) : lo16(av[m][n][j >> 1]);
        float b = rbf(acc[m][n][j]);
        float gj = gg[m][j];
        float r = rbf(gj * a) + rbf(rbf(1.f - gj) * b);
        so[(wr * 32 + m * 16 + 4 * g + j) * 136 + wc * 64 + n * 16 + r16] = (u16)(pk2(r, 0.f) & 0xffffu);
      }
    }
  }
  SYNC();
#pragma unroll
  for (int it = 0; it < 4; ++it) {
    int idx = it * 256 + tid;
    int row = idx >> 4, c8 = idx & 15;
    u32x4 v = *(const u32x4*)(so + row * 136 + c8 * 8);
    *(u32x4*)(out + (size_t)(mt * 64 + row) * D_ + nt * 128 + c8 * 8) = v;
  }
  ST(2, 4);
  asm volatile("s_waitcnt vmcnt(0)" ::: "memory");
  ST(2, 5);
}

extern "C" void kernel_launch(void* const* d_in, const int* in_sizes, int n_in, void* d_out, int out_size, void* d_ws,
                              size_t ws_size, hipStream_t stream) {
  const u16* u = (const u16*)d_in[0];
  const u16* Are = (const u16*)d_in[1];
  const u16* Aim = (const u16*)d_in[2];
  const u16* Bre = (const u16*)d_in[3];
  const u16* Bim = (const u16*)d_in[4];
  const u16* Cre = (const u16*)d_in[5];
  const u16* Cim = (const u16*)d_in[6];
  const u16* logdt = (const u16*)d_in[7];
  const u16* Dskip = (const u16*)d_in[8];
  const u16* wqkv = (const u16*)d_in[10];
  const u16* wo = (const u16*)d_in[11];
  const u16* gw = (const u16*)d_in[12];
  u16* out = (u16*)d_out;
  char* ws = (char*)d_ws;
  float* Kc = (float*)(ws + (32 << 20));
  u16* uT = (u16*)(ws + (4 << 20));
  u16* aT = (u16*)(ws + (8 << 20));
  u16* pq = (u16*)(ws + (12 << 20));
  float* kvp = (float*)(ws + (16 << 20));
  u16* o = (u16*)(ws + (20 << 20));
  float* gate = (float*)(ws + (24 << 20));
  constexpr int LDS1 = GemmCfg<4, 6, NS1, 1>::LDS;
  constexpr int LDS3 = GemmCfg<2, 4, NS3, 2>::LDS;
  hipFuncSetAttribute((const void*)k_l1, hipFuncAttributeMaxDynamicSharedMemorySize, LDS1);
  hipFuncSetAttribute((const void*)k_l3, hipFuncAttributeMaxDynamicSharedMemorySize, LDS3);
  k_l1<<<256, 768, LDS1, stream>>>(u, wqkv, Are, Aim, Bre, Bim, Cre, Cim, logdt, gw, Kc, gate, uT, pq, kvp);
  k_l2<<<1024, 256, CONV_LDS, stream>>>(uT, Kc, Dskip, aT, pq, kvp, o);
  k_l3<<<256, 512, LDS3, stream>>>(o, wo, aT, gate, out);
}
```

```cpp
#include <hip/hip_runtime.h>
#include <stdint.h>

typedef unsigned short u16;
typedef __attribute__((ext_vector_type(8))) short bf16x8;
typedef __attribute__((ext_vector_type(4))) short s16x4;
typedef __attribute__((ext_vector_type(4))) float f32x4;
typedef __attribute__((ext_vector_type(2))) float f32x2;
typedef __attribute__((ext_vector_type(2))) __bf16 bf16x2_t;
typedef __attribute__((ext_vector_type(4))) unsigned u32x4;
typedef __attribute__((ext_vector_type(2))) unsigned u32x2;
typedef __attribute__((address_space(3))) s16x4* lds_s16x4_ptr;

#define NOPK __attribute__((target("no-packed-fp32-ops")))
#define DI __device__ __forceinline__ NOPK
#define U2F(x) __builtin_bit_cast(float, (unsigned)(x))
#define F2U(x) __builtin_bit_cast(unsigned, (float)(x))
#define SYNC()                                                 \
  do {                                                         \
    __builtin_amdgcn_fence(__ATOMIC_RELEASE, "workgroup");     \
    __builtin_amdgcn_s_barrier();                              \
    __builtin_amdgcn_fence(__ATOMIC_ACQUIRE, "workgroup");     \
  } while (0)
#define MFMA16(a, b, c) __builtin_amdgcn_mfma_f32_16x16x32_bf16((a), (b), (c), 0, 0, 0)

#define ST(k, i)
#define S_ 2048
#define D_ 1024
#define NST 128
#define KCW 648

DI float bf2f(u16 v) { return U2F(((unsigned)v) << 16); }
DI unsigned pk2(float lo, float hi) {
  f32x2 f = {lo, hi};
  bf16x2_t b = __builtin_convertvector(f, bf16x2_t);
  return __builtin_bit_cast(unsigned, b);
}
DI float rbf(float f) { return U2F(pk2(f, 0.f) << 16); }
DI float lo16(unsigned p) { return U2F(p << 16); }
DI float hi16(unsigned p) { return U2F(p & 0xffff0000u); }

DI float fexp(float x) {
  float t = x * 1.44269504088896340736f;
  float e = __builtin_fmaf(x, 1.44269504088896340736f, -t) + x * 1.92596299112661746e-8f;
  return __builtin_amdgcn_exp2f(t) * (1.f + e * 0.69314718056f);
}
DI float shx(float v, int m) {
  int lane = threadIdx.x & 63;
  return U2F(__builtin_amdgcn_ds_bpermute((lane ^ m) << 2, (int)F2U(v)));
}
DI float phi_f(float t) {
  float r = t > 0.f ? rbf(t + 1.f) : rbf(fexp(t));
  return rbf(r + 1e-6f);
}

DI unsigned phi_pk(unsigned p) {
  float t0 = lo16(p), t1 = hi16(p);
  float e0 = __builtin_amdgcn_exp2f(t0 * 1.44269504088896340736f), e1 = __builtin_amdgcn_exp2f(t1 * 1.44269504088896340736f);
  float r0 = t0 > 0.f ? t0 + 1.f : e0, r1 = t1 > 0.f ? t1 + 1.f : e1;
  unsigned q = pk2(r0, r1);
  return pk2(lo16(q) + 1e-6f, hi16(q) + 1e-6f);
}

DI void cexp_fast(float xr, float xi, float& o_r, float& o_i) {
  float e = __builtin_amdgcn_exp2f(xr * 1.44269504088896340736f);
  float rev = xi * 0.15915494309189533577f;
  rev = __builtin_amdgcn_fractf(rev);
  o_r = e * __builtin_amdgcn_cosf(rev);
  o_i = e * __builtin_amdgcn_sinf(rev);
}

DI void split8(const float* x, bf16x8& hi, bf16x8& lo) {
  u32x4 h, l;
#pragma unroll
  for (int i = 0; i < 4; ++i) {
    unsigned ph = pk2(x[2 * i], x[2 * i + 1]);
    h[i] = ph;
    l[i] = pk2(x[2 * i] - lo16(ph), x[2 * i + 1] - hi16(ph));
  }
  hi = __builtin_bit_cast(bf16x8, h);
  lo = __builtin_bit_cast(bf16x8, l);
}

DI int krow_l0(int tile, int row) {
  if (tile == 0) return (row & 7) + 128 * (row >> 3);
  if (tile == 1) return row < 8 ? 256 + 2 * (row & 3) + 128 * (row >> 2) : 512 + 4 * (row & 1) + 128 * ((row - 8) >> 1);
  return row < 8 ? 1024 + 128 * row : (row == 8 ? 2048 : 0);
}
DI int kmap(int l) {
  int rl = (int)rbf((float)l);
  int r = 24 - __builtin_clz(rl | 128);
  r = r > 3 ? 3 : r;
  int R = 8 * r + ((rl & 7) >> r) + ((rl >> 7) << (3 - r));
  return 16 * R + ((rl >> 3) & 15);
}
struct KgenState {
  const u16 *Are, *Aim, *Bre, *Bim, *Cre, *Cim;
  float* Kc;
  int d;
  float dt, fl[3], fb;
  unsigned pv[12], pn[12];
  float xb[8], xa[3][8];
  f32x4 acc[3];
};
DI void kg_init(KgenState& k, int lane, const u16* logdt) {
  k.dt = rbf(fexp(bf2f(logdt[k.d])));
  const int a = lane & 15;
#pragma unroll
  for (int r = 0; r < 3; ++r) { k.fl[r] = (float)krow_l0(r, a); k.acc[r] = (f32x4){0.f, 0.f, 0.f, 0.f}; }
  k.fb = (float)(8 * a);
}
DI void kg_load1(unsigned& o0, unsigned& o1, const u16* arr, int off, int g) {
  const unsigned* p = (const unsigned*)(arr + off);
  unsigned v0 = __builtin_amdgcn_readfirstlane(p[0]), v1 = __builtin_amdgcn_readfirstlane(p[1]);
  unsigned v2 = __builtin_amdgcn_readfirstlane(p[2]), v3 = __builtin_amdgcn_readfirstlane(p[3]);
  unsigned v4 = __builtin_amdgcn_readfirstlane(p[4]), v5 = __builtin_amdgcn_readfirstlane(p[5]);
  unsigned v6 = __builtin_amdgcn_readfirstlane(p[6]), v7 = __builtin_amdgcn_readfirstlane(p[7]);
  unsigned x0 = (g & 1) ? v2 : v0, y0 = (g & 1) ? v6 : v4;
  unsigned x1 = (g & 1) ? v3 : v1, y1 = (g & 1) ? v7 : v5;
  o0 = (g & 2) ? y0 : x0;
  o1 = (g & 2) ? y1 : x1;
}
DI void kg_load(KgenState& k, int ks, int g) {
  const int off = k.d * NST + 16 * ks;
  kg_load1(k.pv[0], k.pv[1], k.Are, off, g);
  kg_load1(k.pv[2], k.pv[3], k.Aim, off, g);
  kg_load1(k.pv[4], k.pv[5], k.Bre, off, g);
  kg_load1(k.pv[6], k.pv[7], k.Bim, off, g);
  kg_load1(k.pv[8], k.pv[9], k.Cre, off, g);
  kg_load1(k.pv[10], k.pv[11], k.Cim, off, g);
}
DI void kg_loadv(KgenState& k, int ks, int g) {
  const int off = k.d * NST + 16 * ks + 4 * g;
  u32x2 t;
  t = *(const u32x2*)(k.Are + off); k.pn[0] = t[0]; k.pn[1] = t[1];
  t = *(const u32x2*)(k.Aim + off); k.pn[2] = t[0]; k.pn[3] = t[1];
  t = *(const u32x2*)(k.Bre + off); k.pn[4] = t[0]; k.pn[5] = t[1];
  t = *(const u32x2*)(k.Bim + off); k.pn[6] = t[0]; k.pn[7] = t[1];
  t = *(const u32x2*)(k.Cre + off); k.pn[8] = t[0]; k.pn[9] = t[1];
  t = *(const u32x2*)(k.Cim + off); k.pn[10] = t[0]; k.pn[11] = t[1];
}
DI void cexp_pre(float xr2, float xi2, float& o_r, float& o_i) {
  float e = __builtin_amdgcn_exp2f(xr2);
  float rev = __builtin_amdgcn_fractf(xi2);
  o_r = e * __builtin_amdgcn_cosf(rev);
  o_i = e * __builtin_amdgcn_sinf(rev);
}
#define MFMA4F(a, b, c) __builtin_amdgcn_mfma_f32_16x16x4f32((a), (b), (c), 0, 0, 0)
template <int I>
DI void kg_quarter(KgenState& k) {
#define KEX(t) ((I & 1) ? hi16(k.pv[2 * (t) + (I >> 1)]) : lo16(k.pv[2 * (t) + (I >> 1)]))
  const float ar = k.dt * KEX(0), ai = k.dt * KEX(1), br = k.dt * KEX(2), bi = k.dt * KEX(3), c_r = KEX(4), c_i = KEX(5);
#undef KEX
  const float cr = c_r * br - c_i * bi, ci = c_r * bi + c_i * br;
  const float ar2 = ar * 1.44269504088896340736f, ai2 = ai * 0.15915494309189533577f;
  float pr, pi;
  cexp_pre(ar2 * k.fb, ai2 * k.fb, pr, pi);
  k.xb[2 * I] = cr * pr - ci * pi;
  k.xb[2 * I + 1] = -(cr * pi + ci * pr);
#pragma unroll
  for (int r = 0; r < 3; ++r) cexp_pre(ar2 * k.fl[r], ai2 * k.fl[r], k.xa[r][2 * I], k.xa[r][2 * I + 1]);
}
DI void kg_finish(KgenState& k) {
  bf16x8 bh, bl;
  split8(k.xb, bh, bl);
#pragma unroll
  for (int r = 0; r < 3; ++r) {
    bf16x8 ah, al;
    split8(k.xa[r], ah, al);
    k.acc[r] = MFMA16(ah, bh, k.acc[r]);
    k.acc[r] = MFMA16(ah, bl, k.acc[r]);
    k.acc[r] = MFMA16(al, bh, k.acc[r]);
  }
}
DI void kg_store(KgenState& k, int lane) {
  float* out = k.Kc + (size_t)k.d * 768;
#pragma unroll
  for (int r = 0; r < 3; ++r)
#pragma unroll
    for (int j = 0; j < 4; ++j) __builtin_nontemporal_store(k.acc[r][j], out + r * 256 + 16 * (4 * (lane >> 4) + j) + (lane & 15));
}
struct NoSide {
  static constexpr bool kHas = false;
  DI void begin(int) {}
  DI void even(int, int) {}
  DI void odd() {}
  DI void end(int, int) {}
};
struct L1Side {
  static constexpr bool kHas = true;
  KgenState k;
  const u16 *logdt, *u, *gw;
  float* gate;
  int row0;
  DI void begin(int lane) { kg_loadv(k, 0, lane >> 4); kg_init(k, lane, logdt); }
  DI void even(int ks, int lane) {
#pragma unroll
    for (int j = 0; j < 12; ++j) k.pv[j] = k.pn[j];
    kg_loadv(k, (ks + 1) & 7, lane >> 4);
    kg_quarter<0>(k); kg_quarter<1>(k);
  }
  DI void odd() { kg_quarter<2>(k); kg_quarter<3>(k); kg_finish(k); }
  DI void end(int wid8, int lane) {
    kg_store(k, lane);
    u32x4 w0 = *(const u32x4*)(gw + lane * 16), w1 = *(const u32x4*)(gw + lane * 16 + 8);
#pragma unroll
    for (int rr = 0; rr < 2; ++rr) {
      const int row = row0 + (wid8 - 8) * 2 + rr;
      u32x4 x0 = *(const u32x4*)(u + (size_t)row * D_ + lane * 16), x1 = *(const u32x4*)(u + (size_t)row * D_ + lane * 16 + 8);
      float s = 0.f;
#pragma unroll
      for (int j = 0; j < 4; ++j) {
        s += rbf(lo16(x0[j]) * lo16(w0[j])) + rbf(hi16(x0[j]) * hi16(w0[j]));
        s += rbf(lo16(x1[j]) * lo16(w1[j])) + rbf(hi16(x1[j]) * hi16(w1[j]));
      }
#pragma unroll
      for (int oo = 32; oo > 0; oo >>= 1) s += shx(s, oo);
      float x = rbf(s);
      float gg = rbf(1.f / (1.f + fexp(-x)));
      if (lane == 0) gate[row] = gg;
    }
  }
};

template <int MT, int NT, int NS, int UPS>
struct GemmCfg {
  static constexpr int BM = 32 * MT;
  static constexpr int NCG = NT;
  static constexpr int A_BYTES = BM * 128;
  static constexpr int B_BYTES = NCG * 8 * 512;
  static constexpr int UNIT = A_BYTES + B_BYTES;
  static constexpr int STAGE = UPS * UNIT;
  static constexpr int NSTG = 16 / UPS;
  static constexpr int G = UPS * (MT + NT);
  static constexpr int LDS = NS * STAGE;
};

struct NoHook {
  DI void run(int, const char*) const {}
};

template <int MT, int NT, int NS, int UPS, int LDB>
struct GemmOps {
  using C = GemmCfg<MT, NT, NS, UPS>;
  static constexpr int NCG = C::NCG;
  DI static bf16x8 reada(const char* sb, int j, const unsigned (&aoff)[2], int m) {
    return *(const bf16x8*)(sb + (j >> 1) * C::UNIT + aoff[j & 1] + m * 2048);
  }
  DI static bf16x8 readb(const char* sb, int j, const unsigned (&boff)[2][2], int n) {
    const char* p = sb + (j >> 1) * C::UNIT + (4 * (j & 1) * NCG + (n >> 1)) * 512;
    s16x4 t0 = __builtin_amdgcn_ds_read_tr16_b64_v4i16((lds_s16x4_ptr)(p + boff[n & 1][0]));
    s16x4 t1 = __builtin_amdgcn_ds_read_tr16_b64_v4i16((lds_s16x4_ptr)(p + boff[n & 1][1]));
    return __builtin_shufflevector(t0, t1, 0, 1, 2, 3, 4, 5, 6, 7);
  }
  DI static void issue(char* sbase, int ku0, int wid, int lane, const u16* const (&asrc)[MT], const u16* const (&bsrc)[NT]) {
#pragma unroll
    for (int uu = 0; uu < UPS; ++uu) {
      const int ku = (ku0 + uu) & 15;
      char* sa = sbase + uu * C::UNIT;
      char* sb = sa + C::A_BYTES;
#pragma unroll
      for (int i = 0; i < MT; ++i)
        __builtin_amdgcn_global_load_lds((const unsigned*)(asrc[i] + ku * 64), (unsigned*)(sa + (wid * MT + i) * 1024 + lane * 16), 16, 0, 0);
#pragma unroll
      for (int i = 0; i < NT; ++i)
        __builtin_amdgcn_global_load_lds((const unsigned*)(bsrc[i] + (size_t)ku * 64 * LDB), (unsigned*)(sb + (wid * NT + i) * 1024 + lane * 16), 16, 0, 0);
    }
  }
};

template <int MT, int NT, int NS, int UPS, int LDB, typename ColFn, typename Hook, typename Side>
DI bool gemm_core(const u16* __restrict__ A, const u16* __restrict__ B, ColFn colfn, char* smem, f32x4 (&acc)[MT][NT],
                  const Hook& hook, int rot, Side& side) {
  using C = GemmCfg<MT, NT, NS, UPS>;
  using O = GemmOps<MT, NT, NS, UPS, LDB>;
  constexpr int KID = (MT == 4) ? 0 : 2;
  constexpr int NCG = C::NCG;
  constexpr int NSTG = C::NSTG;
  const int tid = threadIdx.x, lane = tid & 63, wid8 = __builtin_amdgcn_readfirstlane(tid >> 6);
  const int wid = wid8 & 3;
  if (Side::kHas && wid8 >= 8) {
    side.begin(lane);
#pragma unroll 1
    for (int ks = 0; ks < NSTG / 2; ++ks) {
      side.even(ks, lane);
      asm volatile("s_barrier" ::: "memory");
      side.odd();
      asm volatile("s_barrier" ::: "memory");
    }
    asm volatile("s_barrier" ::: "memory");
    side.end(wid8, lane);
    return false;
  }
  if (wid8 >= 4) {
    const u16* asrc[MT];
#pragma unroll
    for (int i = 0; i < MT; ++i) {
      int P = (wid * MT + i) * 64 + lane;
      int line = P >> 4, slot = P & 15;
      int row = 2 * line + (slot >> 3), chunk = (slot & 7) ^ (line & 7);
      asrc[i] = A + (size_t)row * D_ + chunk * 8;
    }
    const u16* bsrc[NT];
#pragma unroll
    for (int i = 0; i < NT; ++i) {
      int ins = wid * NT + i;
      int kb = ins / (NCG / 2), cgp = ins % (NCG / 2);
      int sub = lane >> 5, r7 = (lane & 31) >> 2, chp = lane & 3;
      int row = kb * 8 + r7;
      int ch = chp ^ ((row >> 2) & 3);
      bsrc[i] = B + (size_t)row * LDB + colfn(cgp * 2 + sub) + ch * 8;
    }
#pragma unroll 1
    for (int st = 0; st < NS; ++st) O::issue(smem + st * C::STAGE, st * UPS + rot, wid, lane, asrc, bsrc);
    asm volatile("s_waitcnt vmcnt(%0)\n\ts_barrier" ::"i"(C::G * (NS - 1)) : "memory");
    int buf = 0;
#pragma unroll 1
    for (int s = 0; s + NS < NSTG; ++s) {
      asm volatile("s_waitcnt vmcnt(%0)\n\ts_barrier" ::"i"(C::G * (NS - 2)) : "memory");
      O::issue(smem + buf * C::STAGE, (s + NS) * UPS + rot, wid, lane, asrc, bsrc);
      buf = (buf + 1 == NS) ? 0 : buf + 1;
    }
    if constexpr (NS >= 4) asm volatile("s_waitcnt vmcnt(%0)\n\ts_barrier" ::"i"(C::G * 2) : "memory");
    if constexpr (NS >= 3) asm volatile("s_waitcnt vmcnt(%0)\n\ts_barrier" ::"i"(C::G * 1) : "memory");
    asm volatile("s_waitcnt vmcnt(0)\n\ts_barrier" ::: "memory");
    asm volatile("s_barrier" ::: "memory");
    return false;
  }
  const int wr = wid >> 1, wc = wid & 1;
  const int r16 = lane & 15, g = lane >> 4;
  unsigned aoff[2];
  {
    int row = wr * (16 * MT) + r16;
#pragma unroll
    for (int ks = 0; ks < 2; ++ks) aoff[ks] = (row >> 1) * 256 + (((row & 1) * 8) + ((4 * ks + g) ^ ((row >> 1) & 7))) * 16;
  }
  unsigned boff[2][2];
  {
    int q = r16 >> 2, p = lane & 3;
#pragma unroll
    for (int half = 0; half < 2; ++half)
#pragma unroll
      for (int t = 0; t < 2; ++t)
        boff[half][t] = C::A_BYTES + (g * NCG + wc * (NT / 2)) * 512 + (4 * t + q) * 64 +
                        ((2 * (half ^ (g & 1))) + ((p >> 1) ^ t)) * 16 + 8 * (p & 1);
  }
#pragma unroll
  for (int m = 0; m < MT; ++m)
#pragma unroll
    for (int n = 0; n < NT; ++n) acc[m][n] = (f32x4){0.f, 0.f, 0.f, 0.f};
  ST(KID, 0);
  ST(KID, 1);
  bf16x8 af[2][MT], bfr[NT];
  asm volatile("s_barrier" ::: "memory");
  ST(KID, 2);
#pragma unroll
  for (int m = 0; m < MT; ++m) af[0][m] = O::reada(smem, 0, aoff, m);
#pragma unroll
  for (int n = 0; n < NT; ++n) bfr[n] = O::readb(smem, 0, boff, n);
  int buf = 0;
#pragma unroll 1
  for (int s = 0; s < NSTG; ++s) {
    const char* sb = smem + buf * C::STAGE;
    const int nbuf = (buf + 1 == NS) ? 0 : buf + 1;
    const char* sn = smem + nbuf * C::STAGE;
    const bool more = s + 1 < NSTG;
#pragma unroll
    for (int uu = 0; uu < UPS; ++uu) hook.run((s * UPS + uu + rot) & 15, sb + uu * C::UNIT);
#pragma unroll
    for (int j = 0; j < 2 * UPS; ++j) {
      constexpr int LAST = 2 * UPS - 1;
      if (j < LAST) {
#pragma unroll
        for (int m = 0; m < MT; ++m) af[(j + 1) & 1][m] = O::reada(sb, j + 1, aoff, m);
#pragma unroll
        for (int n = 0; n < NT; ++n) {
#pragma unroll
          for (int m = 0; m < MT; ++m) acc[m][n] = MFMA16(af[j & 1][m], bfr[n], acc[m][n]);
          bfr[n] = O::readb(sb, j + 1, boff, n);
        }
      } else {
        asm volatile("s_waitcnt lgkmcnt(0)\n\ts_barrier" ::: "memory");
#pragma unroll
        for (int m = 0; m < MT; ++m) af[0][m] = O::reada(sn, 0, aoff, m);
#pragma unroll
        for (int n = 0; n < NT; ++n) {
#pragma unroll
          for (int m = 0; m < MT; ++m) acc[m][n] = MFMA16(af[1][m], bfr[n], acc[m][n]);
          bfr[n] = O::readb(sn, 0, boff, n);
        }
      }
    }
    (void)more;
    buf = nbuf;
  }
  asm volatile("s_waitcnt lgkmcnt(0)" ::: "memory");
  ST(KID, 3);
  return true;
}

struct QkvCol {
  int hp;
  DI int operator()(int cg) const { return (cg % 3) * 1024 + hp * 64 + (cg / 3) * 32; }
};
struct TrHook {
  int hp, mt;
  u16* uT;
  DI void run(int ku, const char* sbuf) const {
    if (ku == hp) {
      const int tid = threadIdx.x;
      const int c = tid & 63, tg = tid >> 6;
      u16* dst = uT + (size_t)(ku * 64 + c) * S_ + mt * 128 + tg * 32;
#pragma unroll 1
      for (int hh = 0; hh < 4; ++hh) {
        unsigned w[4];
#pragma unroll
        for (int j = 0; j < 4; ++j) {
          int r0 = tg * 32 + 8 * hh + 2 * j;
          const char* p = sbuf + (r0 >> 1) * 256 + (((c >> 3) ^ ((r0 >> 1) & 7)) * 16) + (c & 7) * 2;
          unsigned a0 = *(const u16*)p, a1 = *(const u16*)(p + 128);
          w[j] = a0 | (a1 << 16);
        }
        __builtin_nontemporal_store((u32x4){w[0], w[1], w[2], w[3]}, (u32x4*)(dst + 8 * hh));
      }
    }
  }
};

#define NS1 4
__global__ __launch_bounds__(768, 3) NOPK void k_l1(const u16* __restrict__ u, const u16* __restrict__ wqkv,
                                                    const u16* __restrict__ Are, const u16* __restrict__ Aim,
                                                    const u16* __restrict__ Bre, const u16* __restrict__ Bim,
                                                    const u16* __restrict__ Cre, const u16* __restrict__ Cim,
                                                    const u16* __restrict__ logdt, const u16* __restrict__ gw,
                                                    float* __restrict__ Kc, float* __restrict__ gate, u16* __restrict__ uT, u16* __restrict__ pq, float* __restrict__ kvp) {
  extern __shared__ __attribute__((aligned(16))) char smem[];
  const int bid = blockIdx.x;
  const int tid = threadIdx.x, lane = tid & 63, wid = __builtin_amdgcn_readfirstlane(tid >> 6);
  const int xcd = bid & 7, i = bid >> 3;
  const int mt = (xcd & 3) * 4 + (i & 3), hp = (xcd >> 2) * 8 + (i >> 2);
  f32x4 acc[4][6];
  QkvCol cf{hp};
  const int rot = i & 15;
  TrHook hook{hp, mt, uT};
  L1Side side;
  side.k.Are = Are; side.k.Aim = Aim; side.k.Bre = Bre; side.k.Bim = Bim; side.k.Cre = Cre; side.k.Cim = Cim;
  side.k.Kc = Kc; side.k.d = bid * 4 + (wid & 3); side.logdt = logdt;
  side.u = u; side.gw = gw; side.gate = gate; side.row0 = mt * 128 + hp * 8;
  if (!gemm_core<4, 6, NS1, 1, 3072>(u + (size_t)mt * 128 * D_, wqkv, cf, smem, acc, hook, rot, side)) return;
  const int wr = (wid >> 1) & 1, wc = wid & 1, r16 = lane & 15, g = lane >> 4;
  u16* sq = (u16*)smem;
#pragma unroll
  for (int m = 0; m < 4; ++m)
#pragma unroll
    for (int n = 0; n < 2; ++n) {
      unsigned p0 = pk2(acc[m][n][0], acc[m][n][1]), p1 = pk2(acc[m][n][2], acc[m][n][3]);
      u16* dst = sq + (wr * 64 + m * 16 + 4 * g) * 72 + wc * 32 + n * 16 + r16;
      dst[0] = (u16)p0; dst[72] = (u16)(p0 >> 16); dst[144] = (u16)p1; dst[216] = (u16)(p1 >> 16);
    }
  f32x4 kvacc[2][2], zacc[2];
#pragma unroll
  for (int x = 0; x < 2; ++x) {
    zacc[x] = (f32x4){0.f, 0.f, 0.f, 0.f};
#pragma unroll
    for (int y = 0; y < 2; ++y) kvacc[x][y] = (f32x4){0.f, 0.f, 0.f, 0.f};
  }
  bf16x8 ones;
#pragma unroll
  for (int j = 0; j < 8; ++j) ones[j] = (short)0x3f80;
#pragma unroll
  for (int mp = 0; mp < 2; ++mp) {
    bf16x8 kf[2], vf[2];
#pragma unroll
    for (int x = 0; x < 2; ++x) {
      u32x4 pk_, pv_;
#pragma unroll
      for (int h = 0; h < 2; ++h) {
        const f32x4 ka = acc[2 * mp + h][2 + x], va = acc[2 * mp + h][4 + x];
        pk_[2 * h] = phi_pk(pk2(ka[0], ka[1]));
        pk_[2 * h + 1] = phi_pk(pk2(ka[2], ka[3]));
        pv_[2 * h] = pk2(va[0], va[1]);
        pv_[2 * h + 1] = pk2(va[2], va[3]);
      }
      kf[x] = __builtin_bit_cast(bf16x8, pk_);
      vf[x] = __builtin_bit_cast(bf16x8, pv_);
    }
#pragma unroll
    for (int x = 0; x < 2; ++x) {
#pragma unroll
      for (int y = 0; y < 2; ++y) kvacc[x][y] = MFMA16(kf[x], vf[y], kvacc[x][y]);
      zacc[x] = MFMA16(kf[x], ones, zacc[x]);
    }
  }
  float* sred = (float*)(smem + 20480) + wc * 1056;
  if (wr == 1) {
#pragma unroll
    for (int x = 0; x < 2; ++x) {
#pragma unroll
      for (int y = 0; y < 2; ++y)
#pragma unroll
        for (int j = 0; j < 4; ++j) sred[(x * 16 + 4 * g + j) * 32 + y * 16 + r16] = kvacc[x][y][j];
      if (r16 == 0)
#pragma unroll
        for (int j = 0; j < 4; ++j) sred[1024 + x * 16 + 4 * g + j] = zacc[x][j];
    }
  }
  SYNC();
  if (wr == 0) {
    float* dst = kvp + ((size_t)(hp * 2 + wc) * 16 + mt) * 1056;
#pragma unroll
    for (int x = 0; x < 2; ++x) {
#pragma unroll
      for (int y = 0; y < 2; ++y)
#pragma unroll
        for (int j = 0; j < 4; ++j) {
          int idx = (x * 16 + 4 * g + j) * 32 + y * 16 + r16;
          __builtin_nontemporal_store(kvacc[x][y][j] + sred[idx], dst + idx);
        }
      if (r16 == 0)
#pragma unroll
        for (int j = 0; j < 4; ++j) {
          int idx = 1024 + x * 16 + 4 * g + j;
          dst[idx] = zacc[x][j] + sred[idx];
        }
    }
  }
#pragma unroll
  for (int it = 0; it < 4; ++it) {
    int idx = it * 256 + tid;
    int row = idx >> 3, c8 = idx & 7;
    u32x4 v = *(const u32x4*)(sq + row * 72 + c8 * 8);
    __builtin_nontemporal_store(v, (u32x4*)(pq + (size_t)(mt * 128 + row) * D_ + hp * 64 + c8 * 8));
  }
  ST(0, 4);
  asm volatile("s_waitcnt vmcnt(0)" ::: "memory");
  ST(0, 5);
}

#define TC_LEN 2080
#define TC_BYTES (TC_LEN * 2)
#define CONV_U_OFF (4 * TC_BYTES)
#define CONV_KC_OFF (CONV_U_OFF + 4640)
#define CONV_LDS (CONV_KC_OFF + 3072)

__global__ __launch_bounds__(256, 4) NOPK void k_l2(const u16* __restrict__ uT,
                                                    const float* __restrict__ Kg, const u16* __restrict__ Dskip,
                                                    u16* __restrict__ aT,
                                                    const u16* __restrict__ pq, const float* __restrict__ kvp,
                                                    u16* __restrict__ o) {
  extern __shared__ __attribute__((aligned(16))) char smem[];
  const int bid = blockIdx.x;
  const int tid = threadIdx.x, lane = tid & 63, wid = __builtin_amdgcn_readfirstlane(tid >> 6);
  const int r16 = lane & 15, g = lane >> 4;
  ST(1, 0);
  const u32x4 uv = *(const u32x4*)(uT + (size_t)bid * S_ + tid * 8);
  f32x4 kq = (f32x4){0.f, 0.f, 0.f, 0.f};
  if (tid < 192) kq = *(const f32x4*)(Kg + (size_t)bid * 768 + tid * 4);
  if (bid < 256) {
    const int h = bid >> 3, rt = bid & 7;
    u16* kvT = (u16*)smem;
    u16* zs = (u16*)(smem + 4096);
    u32x4 bqv[4];
#pragma unroll
    for (int tt = 0; tt < 4; ++tt)
      bqv[tt] = *(const u32x4*)(pq + (size_t)(rt * 256 + wid * 64 + tt * 16 + r16) * D_ + h * 32 + 8 * g);
    const float* src = kvp + (size_t)h * 16 * 1056;
    {
      f32x4 pv4[16];
      f32x4 zq[16];
#pragma unroll
      for (int p = 0; p < 16; ++p) pv4[p] = *(const f32x4*)(src + p * 1056 + tid * 4);
#pragma unroll
      for (int p = 0; p < 16; ++p) zq[p] = *(const f32x4*)(src + p * 1056 + 1024 + (tid & 7) * 4);
      f32x4 zv = zq[0];
#pragma unroll
      for (int p = 1; p < 16; ++p) zv += zq[p];
      f32x4 sv = pv4[0];
#pragma unroll
      for (int p = 1; p < 16; ++p) sv += pv4[p];
      const int dd = tid >> 3, e0 = (tid & 7) * 4;
#pragma unroll
      for (int j = 0; j < 4; ++j) kvT[(e0 + j) * 40 + dd] = (u16)(pk2(sv[j], 0.f) & 0xffffu);
      if (tid < 8) {
        u32x2 zz; zz[0] = pk2(zv[0], zv[1]); zz[1] = pk2(zv[2], zv[3]);
        *(u32x2*)(zs + tid * 4) = zz;
      }
    }
    SYNC();
    bf16x8 ka0 = *(const bf16x8*)(kvT + r16 * 40 + 8 * g);
    bf16x8 ka1 = *(const bf16x8*)(kvT + (16 + r16) * 40 + 8 * g);
    bf16x8 za = *(const bf16x8*)(zs + 8 * g);
#pragma unroll
    for (int tt = 0; tt < 4; ++tt) {
      int tok = rt * 256 + wid * 64 + tt * 16 + r16;
      u32x4 pv;
#pragma unroll
      for (int j = 0; j < 4; ++j) pv[j] = phi_pk(bqv[tt][j]);
      bf16x8 bq = __builtin_bit_cast(bf16x8, pv);
      f32x4 zero = (f32x4){0.f, 0.f, 0.f, 0.f};
      f32x4 n0 = MFMA16(ka0, bq, zero), n1 = MFMA16(ka1, bq, zero), dn = MFMA16(za, bq, zero);
      float den = rbf(rbf(dn[0]) + 1e-6f);
      u32x2 o0, o1;
      o0[0] = pk2(rbf(n0[0]) / den, rbf(n0[1]) / den); o0[1] = pk2(rbf(n0[2]) / den, rbf(n0[3]) / den);
      o1[0] = pk2(rbf(n1[0]) / den, rbf(n1[1]) / den); o1[1] = pk2(rbf(n1[2]) / den, rbf(n1[3]) / den);
      __builtin_nontemporal_store(o0, (u32x2*)(o + (size_t)tok * D_ + h * 32 + 4 * g));
      __builtin_nontemporal_store(o1, (u32x2*)(o + (size_t)tok * D_ + h * 32 + 16 + 4 * g));
    }
    SYNC();
  }
  ST(1, 1);
  {
    const int d = bid;
    float* Kc = (float*)(smem + CONV_KC_OFF);
    u16* ul = (u16*)(smem + CONV_U_OFF);
    if (tid < 192) {
      u32x4 pk4;
#pragma unroll
      for (int j = 0; j < 4; ++j) {
        unsigned h = pk2(kq[j], 0.f) & 0xffffu;
        pk4[j] = h | (pk2(kq[j] - U2F(h << 16), 0.f) << 16);
      }
      *(u32x4*)(Kc + tid * 4) = pk4;
    }
    *(u32x4*)(ul + 272 + tid * 8) = uv;
    if (tid < 34) *(u32x4*)(ul + tid * 8) = (u32x4){0u, 0u, 0u, 0u};
    SYNC();
    ST(1, 2);
    {
      const int qb = tid;
      unsigned w[9];
#pragma unroll
      for (int j = 0; j < 9; ++j) {
        int l = 2048 - (qb * 8 + j);
        unsigned x = ((const unsigned*)Kc)[kmap(l)];
        w[j] = (l < 2048) ? x : 0u;
      }
#pragma unroll
      for (int c = 0; c < 2; ++c) {
        u32x4 vh, vl;
#pragma unroll
        for (int j = 0; j < 4; ++j) {
          vh[j] = __builtin_amdgcn_perm(w[c + 2 * j + 1], w[c + 2 * j], 0x05040100u);
          vl[j] = __builtin_amdgcn_perm(w[c + 2 * j + 1], w[c + 2 * j], 0x07060302u);
        }
        *(u32x4*)(smem + c * TC_BYTES + qb * 16) = vh;
        *(u32x4*)(smem + (2 + c) * TC_BYTES + qb * 16) = vl;
      }
      const unsigned hw8 = w[8] & 0xffffu, lw8 = w[8] >> 16;
      if (tid == 255) {
#pragma unroll
        for (int c = 0; c < 2; ++c) {
          *(u32x4*)(smem + c * TC_BYTES + 4096) = (u32x4){c == 0 ? hw8 : 0u, 0u, 0u, 0u};
          *(u32x4*)(smem + c * TC_BYTES + 4112) = (u32x4){0u, 0u, 0u, 0u};
          *(u32x4*)(smem + (2 + c) * TC_BYTES + 4096) = (u32x4){c == 0 ? lw8 : 0u, 0u, 0u, 0u};
          *(u32x4*)(smem + (2 + c) * TC_BYTES + 4112) = (u32x4){0u, 0u, 0u, 0u};
        }
      }
    }
    SYNC();
    ST(1, 3);
    f32x4 acc[8];
#pragma unroll
    for (int c = 0; c < 8; ++c) acc[c] = (f32x4){0.f, 0.f, 0.f, 0.f};
    const int cc = r16 & 1;
    const int i0_base = 2048 - 32 * wid - 16 * (g >> 1) + 8 * (g & 1) - r16 - cc;
    const char* abase = smem + cc * TC_BYTES + 2 * i0_base;
    const char* bbase = (const char*)ul + 2 * (272 + 16 * (r16 - (g >> 1)) + 8 * (g & 1)) - 64 * wid;
#pragma unroll
    for (int q = 0; q < 8; ++q) {
#pragma unroll
      for (int h = 0; h < 2; ++h) {
        const int pofs = 8 * q + 4 * h;
        u32x4 vh, vl;
#pragma unroll
        for (int j = 0; j < 4; ++j) {
          vh[j] = *(const unsigned*)(abase - 64 * pofs + 4 * j);
          vl[j] = *(const unsigned*)(abase - 64 * pofs + 2 * TC_BYTES + 4 * j);
        }
        bf16x8 ah = __builtin_bit_cast(bf16x8, vh), al = __builtin_bit_cast(bf16x8, vl);
#pragma unroll
        for (int c = q; c < 8; ++c) {
          bf16x8 bfr = *(const bf16x8*)(bbase + 512 * c - 64 * pofs);
          acc[c] = MFMA16(ah, bfr, acc[c]);
          acc[c] = MFMA16(al, bfr, acc[c]);
        }
      }
    }
    ST(1, 4);
    SYNC();
    float* red = (float*)smem;
    if (wid >= 2) {
#pragma unroll
      for (int c = 0; c < 8; ++c) *(f32x4*)(red + (wid - 2) * 2048 + 16 * (16 * c + r16) + 4 * g) = acc[c];
    }
    SYNC();
    if (wid < 2) {
#pragma unroll
      for (int c = 0; c < 8; ++c) {
        float* p = red + wid * 2048 + 16 * (16 * c + r16) + 4 * g;
        *(f32x4*)p = *(const f32x4*)p + acc[c];
      }
    }
    SYNC();
    {
      const float dsk = bf2f(Dskip[d]);
      f32x4 s0 = *(const f32x4*)(red + tid * 8) + *(const f32x4*)(red + 2048 + tid * 8);
      f32x4 s1 = *(const f32x4*)(red + tid * 8 + 4) + *(const f32x4*)(red + 2048 + tid * 8 + 4);
      u32x4 ov;
#pragma unroll
      for (int j = 0; j < 4; ++j) {
        float y0 = j < 2 ? s0[2 * j] : s1[2 * j - 4], y1 = j < 2 ? s0[2 * j + 1] : s1[2 * j - 3];
        float a0 = rbf(y0) + rbf(lo16(uv[j]) * dsk), a1 = rbf(y1) + rbf(hi16(uv[j]) * dsk);
        ov[j] = pk2(a0, a1);
      }
      __builtin_nontemporal_store(ov, (u32x4*)(aT + (size_t)d * S_ + tid * 8));
    }
    ST(1, 5);
    asm volatile("s_waitcnt vmcnt(0)" ::: "memory");
    ST(1, 6);
  }
}

struct PlainCol {
  int c0;
  DI int operator()(int cg) const { return c0 + cg * 32; }
};
#define NS3 3
__global__ __launch_bounds__(512, 2) NOPK void k_l3(const u16* __restrict__ o, const u16* __restrict__ wo,
                                                    const u16* __restrict__ aT, const float* __restrict__ gate,
                                                    u16* __restrict__ out) {
  extern __shared__ __attribute__((aligned(16))) char smem[];
  const int bid = blockIdx.x;
  const int tid = threadIdx.x, lane = tid & 63, wid = __builtin_amdgcn_readfirstlane(tid >> 6);
  const int xcd = bid & 7, i = bid >> 3;
  const int mt = xcd * 4 + (i & 3), nt = i >> 2;
  const int wr = (wid >> 1) & 1, wc = wid & 1, r16 = lane & 15, g = lane >> 4;
  f32x4 acc[2][4];
  PlainCol cf{nt * 128};
  NoHook hook;
  NoSide noside;
  u32x2 av[2][4];
  f32x4 gg[2];
  if (wid < 4) {
#pragma unroll
    for (int m = 0; m < 2; ++m) {
      int t0 = mt * 64 + wr * 32 + m * 16 + 4 * g;
      gg[m] = *(const f32x4*)(gate + t0);
#pragma unroll
      for (int n = 0; n < 4; ++n) av[m][n] = *(const u32x2*)(aT + (size_t)(nt * 128 + wc * 64 + n * 16 + r16) * S_ + t0);
    }
  }
  if (!gemm_core<2, 4, NS3, 2, 1024>(o + (size_t)mt * 64 * D_, wo, cf, smem, acc, hook, (i & 7) * 2, noside)) return;
  u16* so = (u16*)smem;
#pragma unroll
  for (int m = 0; m < 2; ++m) {
#pragma unroll
    for (int n = 0; n < 4; ++n) {
#pragma unroll
      for (int j = 0; j < 4; ++j) {
        float a = (j & 1) ? hi16(av[m][n][j >> 1]) : lo16(av[m][n][j >> 1]);
        float b = rbf(acc[m][n][j]);
        float gj = gg[m][j];
        float r = rbf(gj * a) + rbf(rbf(1.f - gj) * b);
        so[(wr * 32 + m * 16 + 4 * g + j) * 136 + wc * 64 + n * 16 + r16] = (u16)(pk2(r, 0.f) & 0xffffu);
      }
    }
  }
  SYNC();
#pragma unroll
  for (int it = 0; it < 4; ++it) {
    int idx = it * 256 + tid;
    int row = idx >> 4, c8 = idx & 15;
    u32x4 v = *(const u32x4*)(so + row * 136 + c8 * 8);
    __builtin_nontemporal_store(v, (u32x4*)(out + (size_t)(mt * 64 + row) * D_ + nt * 128 + c8 * 8));
  }
  ST(2, 4);
  asm volatile("s_waitcnt vmcnt(0)" ::: "memory");
  ST(2, 5);
}

extern "C" void kernel_launch(void* const* d_in, const int* in_sizes, int n_in, void* d_out, int out_size, void* d_ws,
                              size_t ws_size, hipStream_t stream) {
  const u16* u = (const u16*)d_in[0];
  const u16* Are = (const u16*)d_in[1];
  const u16* Aim = (const u16*)d_in[2];
  const u16* Bre = (const u16*)d_in[3];
  const u16* Bim = (const u16*)d_in[4];
  const u16* Cre = (const u16*)d_in[5];
  const u16* Cim = (const u16*)d_in[6];
  const u16* logdt = (const u16*)d_in[7];
  const u16* Dskip = (const u16*)d_in[8];
  const u16* wqkv = (const u16*)d_in[10];
  const u16* wo = (const u16*)d_in[11];
  const u16* gw = (const u16*)d_in[12];
  u16* out = (u16*)d_out;
  char* ws = (char*)d_ws;
  float* Kc = (float*)(ws + (32 << 20));
  u16* uT = (u16*)(ws + (4 << 20));
  u16* aT = (u16*)(ws + (8 << 20));
  u16* pq = (u16*)(ws + (12 << 20));
  float* kvp = (float*)(ws + (16 << 20));
  u16* o = (u16*)(ws + (20 << 20));
  float* gate = (float*)(ws + (24 << 20));
  constexpr int LDS1 = GemmCfg<4, 6, NS1, 1>::LDS;
  constexpr int LDS3 = GemmCfg<2, 4, NS3, 2>::LDS;
  hipFuncSetAttribute((const void*)k_l1, hipFuncAttributeMaxDynamicSharedMemorySize, LDS1);
  hipFuncSetAttribute((const void*)k_l3, hipFuncAttributeMaxDynamicSharedMemorySize, LDS3);
  k_l1<<<256, 768, LDS1, stream>>>(u, wqkv, Are, Aim, Bre, Bim, Cre, Cim, logdt, gw, Kc, gate, uT, pq, kvp);
  k_l2<<<1024, 256, CONV_LDS, stream>>>(uT, Kc, Dskip, aT, pq, kvp, o);
  k_l3<<<256, 512, LDS3, stream>>>(o, wo, aT, gate, out);
}
```
